```python
import torch

def _softmax(x, axis=-1):
    return torch.softmax(x, dim=axis)
def _gelu(x):
    return torch.nn.functional.gelu(x, approximate="tanh")
def _silu(x):
    return torch.nn.functional.silu(x)
def _sigmoid(x):
    return torch.sigmoid(x)
def _layernorm(x, axis=-1, eps=1e-5):
    m = x.mean(dim=axis, keepdim=True)
    v = x.var(dim=axis, keepdim=True, unbiased=False)
    return (x - m) / torch.sqrt(v + eps)
def _scatter_add(tgt, idx, src, axis=0):
    return tgt.clone().index_add_(axis, idx, src)
def _segment_sum(src, seg, n):
    out = torch.zeros((n,) + src.shape[1:], dtype=src.dtype)
    return out.index_add_(0, seg, src)
def _pad(x, pw):
    flat = tuple(v for pair in reversed(pw) for v in pair)
    return torch.nn.functional.pad(x, flat)

kernel_name = 'standalone__qk_norm__prompt'

def setup_inputs():

    _w = (lambda a: (lambda t: t.to(torch.bfloat16) if t.is_floating_point() else t)(torch.as_tensor(a)))
    B, H, S, D = 2, 32, 2048, 576
    Dh = D // H
    return {
        "q": _w((lambda *s: torch.randn(*s, dtype=torch.bfloat16) * 0.05)(B, H, S, Dh)),
        "k": _w((lambda *s: torch.randn(*s, dtype=torch.bfloat16) * 0.05)(B, H, S, Dh)),
        "v": _w((lambda *s: torch.randn(*s, dtype=torch.bfloat16) * 0.4)(B, H, S, Dh)),
        "t": _w(torch.abs((lambda *s: torch.randn(*s, dtype=torch.bfloat16) * 0.4)(H)) + 1.0),
    }

def reference(q, k, v, t):
    eps = 1e-6
    qn = q / (torch.sqrt((q * q).sum(axis=-1, keepdims=True)) + eps)
    kn = k / (torch.sqrt((k * k).sum(axis=-1, keepdims=True)) + eps)
    logits = torch.einsum('bhqd,bhkd->bhqk', qn, kn) * t[None, :, None, None]
    attn = _softmax(logits, axis=-1)
    return torch.einsum('bhqk,bhkd->bhqd', attn, v)
```

```cpp
#include <hip/hip_runtime.h>
#include <hip/hip_bf16.h>

typedef __attribute__((ext_vector_type(8))) __bf16 bf16x8;
typedef __attribute__((ext_vector_type(16))) __bf16 bf16x16;
typedef __attribute__((ext_vector_type(4))) short s16x4;
typedef __attribute__((ext_vector_type(8))) float f32x8;
typedef __attribute__((ext_vector_type(16))) float f32x16;
typedef __attribute__((ext_vector_type(32))) float f32x32;
typedef __attribute__((ext_vector_type(2))) unsigned u32x2;
typedef __attribute__((ext_vector_type(4))) unsigned u32x4;
typedef __attribute__((address_space(3))) char* lds_ptr;

#define DI __device__ __forceinline__

constexpr int SEQ = 2048, DH = 18, NHEADS = 32;
constexpr int KM_OFF = 0;
constexpr int KX_OFF = 65536;
constexpr int V_OFF = KX_OFF + 8192;
constexpr int VGRP = 168;
constexpr int LDS_BYTES = V_OFF + 512 * VGRP;
constexpr float LOG2E = 1.4426950408889634f;
constexpr float SHIFT_SAFE = 40.0f;

struct Row9 { unsigned w[9]; };

DI Row9 load_row(const unsigned* p) {
    Row9 r;
    typedef u32x4 __attribute__((aligned(4))) u32x4_u;
    u32x4 a = *(const u32x4_u*)p, b = *(const u32x4_u*)(p + 4);
    r.w[0] = a[0]; r.w[1] = a[1]; r.w[2] = a[2]; r.w[3] = a[3];
    r.w[4] = b[0]; r.w[5] = b[1]; r.w[6] = b[2]; r.w[7] = b[3];
    r.w[8] = p[8];
    return r;
}
DI unsigned cvt_pk(float lo, float hi) {
    typedef __attribute__((ext_vector_type(2))) __bf16 bf16x2;
    typedef __attribute__((ext_vector_type(2))) float f32x2;
    f32x2 f = {lo, hi};
    return __builtin_bit_cast(unsigned, __builtin_convertvector(f, bf16x2));
}
DI Row9 normalise(const Row9& r, float scale) {
    float f[18], ss = 0.f;
#pragma unroll
    for (int i = 0; i < 9; ++i) {
        f[2 * i] = __uint_as_float(r.w[i] << 16);
        f[2 * i + 1] = __uint_as_float(r.w[i] & 0xffff0000u);
        ss = __builtin_fmaf(f[2 * i], f[2 * i], ss);
        ss = __builtin_fmaf(f[2 * i + 1], f[2 * i + 1], ss);
    }
    const float g = scale * __builtin_amdgcn_rcpf(__builtin_amdgcn_sqrtf(ss) + 1e-6f);
    Row9 o;
#pragma unroll
    for (int i = 0; i < 9; ++i) o.w[i] = cvt_pk(f[2 * i] * g, f[2 * i + 1] * g);
    return o;
}
DI void stage_k(lds_ptr lds, int key, const Row9& raw) {
    const Row9 n = normalise(raw, 1.0f);
    lds_ptr p = lds + KM_OFF + (key >> 5) * 1024 + (key & 31) * 16;
    *(__attribute__((address_space(3))) u32x4*)p = (u32x4){n.w[0], n.w[1], n.w[2], n.w[3]};
    *(__attribute__((address_space(3))) u32x4*)(p + 512) = (u32x4){n.w[4], n.w[5], n.w[6], n.w[7]};
    *(__attribute__((address_space(3))) unsigned*)(lds + KX_OFF + key * 4) = n.w[8];
}
DI void stage_v(lds_ptr lds, int key, const Row9& r) {
    typedef __attribute__((address_space(3))) u32x2* p2;
    lds_ptr g = lds + V_OFF + (key >> 2) * VGRP;
    lds_ptr p = g + (key & 3) * 32;
    *(p2)p = (u32x2){r.w[0], r.w[1]};
    *(p2)(p + 8) = (u32x2){r.w[2], r.w[3]};
    *(p2)(p + 16) = (u32x2){r.w[4], r.w[5]};
    *(p2)(p + 24) = (u32x2){r.w[6], r.w[7]};
    *(p2)(g + 128 + (key & 3) * 8) = (u32x2){r.w[8], 0x00003f80u};
    *(p2)(g + 160) = (u32x2){0u, 0u};
}
DI s16x4 tr_read(lds_ptr p) {
    typedef short v4i16_t __attribute__((ext_vector_type(4)));
    return __builtin_amdgcn_ds_read_tr16_b64_v4i16((__attribute__((address_space(3))) v4i16_t*)p);
}
DI bf16x8 v_frag(lds_ptr p) {
    s16x4 lo = tr_read(p), hi = tr_read(p + VGRP);
    return __builtin_bit_cast(bf16x8, __builtin_shufflevector(lo, hi, 0, 1, 2, 3, 4, 5, 6, 7));
}
#define LO16(x) __builtin_shufflevector(x, x, 0, 1, 2, 3, 4, 5, 6, 7, 8, 9, 10, 11, 12, 13, 14, 15)
#define HI16(x) __builtin_shufflevector(x, x, 16, 17, 18, 19, 20, 21, 22, 23, 24, 25, 26, 27, 28, 29, 30, 31)
#define LO8(x) __builtin_bit_cast(bf16x16, __builtin_shufflevector(x, x, 0, 1, 2, 3, 4, 5, 6, 7))
#define HI8(x) __builtin_bit_cast(bf16x16, __builtin_shufflevector(x, x, 8, 9, 10, 11, 12, 13, 14, 15))
#define SMF(a, b, c) __builtin_amdgcn_smfmac_f32_32x32x32_bf16(a, b, c, sel, 0, 0)

template <bool EXACT_MAX>
DI void run_keys(lds_ptr kmp, lds_ptr kxp, lds_ptr vp, int nsteps, bf16x8 qm, s16x4 qx, f32x16& o, float& m, int sel) {
    for (int it = 0; it < nsteps; ++it) {
        const bf16x8 k0 = *(const __attribute__((address_space(3))) bf16x8*)kmp;
        const bf16x8 k1 = *(const __attribute__((address_space(3))) bf16x8*)(kmp + 1024);
        const unsigned kxw = *(const __attribute__((address_space(3))) unsigned*)kxp;
        const s16x4 kx = __builtin_bit_cast(s16x4, (u32x2){kxw, 0x00003f80u});
        const bf16x8 v0 = v_frag(vp), v1 = v_frag(vp + 4 * VGRP), v2 = v_frag(vp + 8 * VGRP), v3 = v_frag(vp + 12 * VGRP);
        f32x32 z;
#pragma unroll
        for (int i = 0; i < 32; ++i) z[i] = 0.f;
        f32x32 s = __builtin_amdgcn_mfma_f32_32x32x4bf16_1k(kx, qx, z, 0, 0, 0);
        f32x16 s0 = __builtin_amdgcn_mfma_f32_32x32x16_bf16(k0, qm, LO16(s), 0, 0, 0);
        f32x16 s1 = __builtin_amdgcn_mfma_f32_32x32x16_bf16(k1, qm, HI16(s), 0, 0, 0);
        if (EXACT_MAX) {
            float mx = __builtin_fmaxf(s0[0], s1[0]);
#pragma unroll
            for (int i = 1; i < 16; ++i) mx = __builtin_fmaxf(mx, __builtin_fmaxf(s0[i], s1[i]));
            mx = __builtin_fmaxf(mx, __shfl_xor(mx, 32));
            const float mn = __builtin_fmaxf(m, mx);
            const float alpha = __builtin_amdgcn_exp2f(m - mn);
            m = mn;
#pragma unroll
            for (int i = 0; i < 16; ++i) { o[i] *= alpha; s0[i] -= mn; s1[i] -= mn; }
        }
#pragma unroll
        for (int i = 0; i < 16; ++i) s0[i] = __builtin_amdgcn_exp2f(s0[i]);
        o = SMF(v0, LO8(s0), o);
        o = SMF(v1, HI8(s0), o);
#pragma unroll
        for (int i = 0; i < 16; ++i) s1[i] = __builtin_amdgcn_exp2f(s1[i]);
        o = SMF(v2, LO8(s1), o);
        o = SMF(v3, HI8(s1), o);
        kmp += 2048; kxp += 256; vp += 16 * VGRP;
    }
}

__global__ __launch_bounds__(1024) void qk_norm_attn(const unsigned* __restrict__ q, const unsigned* __restrict__ k,
                                                     const unsigned* __restrict__ v, const unsigned short* __restrict__ t,
                                                     unsigned* __restrict__ out) {
    __shared__ __attribute__((aligned(16))) char smem[LDS_BYTES];
    lds_ptr lds = (lds_ptr)smem;
    const int tid = threadIdx.x, lane = tid & 63, wid = tid >> 6, r32 = lane & 31, hi = lane >> 5;
    const int b = blockIdx.x;
    const int head = (b & 7) + 8 * (b >> 5), qc = (b >> 3) & 3;
    const size_t hbase = (size_t)head * SEQ * 9;
    const int qrow = qc * 512 + wid * 32 + r32;

    const Row9 qraw = load_row(q + hbase + (size_t)qrow * 9);
    const Row9 k0raw = load_row(k + hbase + (size_t)tid * 9);
    const Row9 v0raw = load_row(v + hbase + (size_t)tid * 9);
    const Row9 k1raw = load_row(k + hbase + (size_t)(tid + 1024) * 9);
    const Row9 v1raw = load_row(v + hbase + (size_t)(tid + 1024) * 9);
    const float c = __uint_as_float((unsigned)t[head & (NHEADS - 1)] << 16) * LOG2E;
    const bool fast = __builtin_fabsf(c) <= SHIFT_SAFE;

    const Row9 qn = normalise(qraw, c);
    const bf16x8 qm = __builtin_bit_cast(bf16x8, hi ? (u32x4){qn.w[4], qn.w[5], qn.w[6], qn.w[7]} : (u32x4){qn.w[0], qn.w[1], qn.w[2], qn.w[3]});
    const unsigned shift_bits = fast ? (cvt_pk(-__builtin_fabsf(c), 0.f) & 0xffffu) : 0u;
    const s16x4 qx = __builtin_bit_cast(s16x4, (u32x2){qn.w[8], shift_bits});

    stage_k(lds, tid, k0raw);
    stage_v(lds, tid, v0raw);
    __syncthreads();

    lds_ptr kmp = lds + KM_OFF + lane * 16;
    lds_ptr kxp = lds + KX_OFF + lane * 4;
    const int i16 = lane & 15, q4 = i16 >> 2, p4 = i16 & 3;
    const int voff = (lane & 16) ? (p4 == 0 ? 128 + q4 * 8 : 160) : q4 * 32 + p4 * 8;
    lds_ptr vp = lds + V_OFF + hi * 2 * VGRP + voff;
    int sel = 0xDDDD;
    asm volatile("" : "+v"(sel));

    f32x16 o;
#pragma unroll
    for (int i = 0; i < 16; ++i) o[i] = 0.f;
    float m = -INFINITY;
    if (fast) run_keys<false>(kmp, kxp, vp, 16, qm, qx, o, m, sel);
    else run_keys<true>(kmp, kxp, vp, 16, qm, qx, o, m, sel);

    stage_k(lds, tid + 1024, k1raw);
    stage_v(lds, tid + 1024, v1raw);
    __syncthreads();

    if (fast) run_keys<false>(kmp + 32768, kxp + 4096, vp + 256 * VGRP, 16, qm, qx, o, m, sel);
    else run_keys<true>(kmp + 32768, kxp + 4096, vp + 256 * VGRP, 16, qm, qx, o, m, sel);

    const float l = __shfl(o[10], r32);
    const float rl = 1.0f / l;
    unsigned* op = out + hbase + (size_t)qrow * 9;
    typedef u32x2 __attribute__((aligned(4))) u32x2_u;
    *(u32x2_u*)(op + 2 * hi) = (u32x2){cvt_pk(o[0] * rl, o[1] * rl), cvt_pk(o[2] * rl, o[3] * rl)};
    *(u32x2_u*)(op + 4 + 2 * hi) = (u32x2){cvt_pk(o[4] * rl, o[5] * rl), cvt_pk(o[6] * rl, o[7] * rl)};
    if (!hi) op[8] = cvt_pk(o[8] * rl, o[9] * rl);
}

extern "C" void kernel_launch(void* const* d_in, const int* in_sizes, int n_in, void* d_out, int out_size, void* d_ws,
                              size_t ws_size, hipStream_t stream) {
    qk_norm_attn<<<256, 1024, 0, stream>>>((const unsigned*)d_in[0], (const unsigned*)d_in[1], (const unsigned*)d_in[2],
                                           (const unsigned short*)d_in[3], (unsigned*)d_out);
}
```

```cpp
#include <hip/hip_runtime.h>
#include <hip/hip_bf16.h>

typedef __attribute__((ext_vector_type(8))) __bf16 bf16x8;
typedef __attribute__((ext_vector_type(16))) __bf16 bf16x16;
typedef __attribute__((ext_vector_type(2))) __bf16 bf16x2;
typedef __attribute__((ext_vector_type(4))) short s16x4;
typedef __attribute__((ext_vector_type(2))) float f32x2;
typedef __attribute__((ext_vector_type(16))) float f32x16;
typedef __attribute__((ext_vector_type(32))) float f32x32;
typedef __attribute__((ext_vector_type(2))) unsigned u32x2;
typedef __attribute__((ext_vector_type(4))) unsigned u32x4;
typedef __attribute__((address_space(3))) char* lds_ptr;
#define LDS_AS(T) __attribute__((address_space(3))) T

#define DI __device__ __forceinline__
#define STAMP_AT(i)
#define STAMP_ARG
#define STAMP_PASS
#define USE2B 1

constexpr int SEQ = 2048, NHEADS = 32;
constexpr int KM_OFF = 0;
constexpr int KX_OFF = 65536;
constexpr int V_OFF = KX_OFF + 8192;
constexpr int VGRP = 168;
constexpr int LDS_BYTES = V_OFF + 512 * VGRP;
constexpr float LOG2E = 1.4426950408889634f;
constexpr float SHIFT_SAFE = 40.0f;
constexpr unsigned ONE_ZERO = 0x00003f80u;

typedef u32x4 __attribute__((aligned(4))) u32x4_u;
typedef u32x2 __attribute__((aligned(4))) u32x2_u;
struct Row9 { u32x4 a, b; unsigned c; };

DI Row9 load_row(const char* base, unsigned off) { Row9 r; const char* p = base + off; r.a = *(const u32x4_u*)p; r.b = *(const u32x4_u*)(p + 16); r.c = *(const unsigned*)(p + 32); return r; }
DI unsigned cvt_pk(float lo, float hi) { f32x2 f = {lo, hi}; return __builtin_bit_cast(unsigned, __builtin_convertvector(f, bf16x2)); }
DI float sq2(unsigned w, float acc) { return __builtin_amdgcn_fdot2_f32_bf16(__builtin_bit_cast(bf16x2, w), __builtin_bit_cast(bf16x2, w), acc, false); }
DI unsigned scale2(unsigned w, float g) { return cvt_pk(__uint_as_float(w << 16) * g, __uint_as_float(w & 0xffff0000u) * g); }
DI float inv_norm(float ss) { return __builtin_amdgcn_rcpf(__builtin_amdgcn_sqrtf(ss) + 1e-6f); }

DI void stage_k(lds_ptr lds, int pos, const Row9& r) {
    float s0 = sq2(r.a[0], 0.f), s1 = sq2(r.a[1], 0.f);
    s0 = sq2(r.a[2], s0); s1 = sq2(r.a[3], s1); s0 = sq2(r.b[0], s0); s1 = sq2(r.b[1], s1);
    s0 = sq2(r.b[2], s0); s1 = sq2(r.b[3], s1); s0 = sq2(r.c, s0);
    const float g = inv_norm(s0 + s1);
    lds_ptr p = lds + KM_OFF + (pos >> 5) * 1024 + (pos & 31) * 16;
    *(LDS_AS(u32x4)*)p = (u32x4){scale2(r.a[0], g), scale2(r.a[1], g), scale2(r.a[2], g), scale2(r.a[3], g)};
    *(LDS_AS(u32x4)*)(p + 512) = (u32x4){scale2(r.b[0], g), scale2(r.b[1], g), scale2(r.b[2], g), scale2(r.b[3], g)};
    *(LDS_AS(unsigned)*)(lds + KX_OFF + pos * 4) = scale2(r.c, g);
}
DI void stage_v(lds_ptr lds, int pos, const Row9& r) {
    lds_ptr g = lds + V_OFF + (pos >> 2) * VGRP;
    lds_ptr p = g + (pos & 3) * 32;
    *(LDS_AS(u32x2)*)p = (u32x2){r.a[0], r.a[1]};
    *(LDS_AS(u32x2)*)(p + 8) = (u32x2){r.a[2], r.a[3]};
    *(LDS_AS(u32x2)*)(p + 16) = (u32x2){r.b[0], r.b[1]};
    *(LDS_AS(u32x2)*)(p + 24) = (u32x2){r.b[2], r.b[3]};
    *(LDS_AS(u32x2)*)(g + 128 + (pos & 3) * 8) = (u32x2){r.c, ONE_ZERO};
    *(LDS_AS(u32x2)*)(g + 160) = (u32x2){0u, 0u};
}
DI s16x4 tr_read(lds_ptr p) {
    typedef short v4i16_t __attribute__((ext_vector_type(4)));
    return __builtin_amdgcn_ds_read_tr16_b64_v4i16((LDS_AS(v4i16_t)*)p);
}
DI bf16x8 v_frag(lds_ptr p) {
    s16x4 lo = tr_read(p), hi = tr_read(p + VGRP);
    return __builtin_bit_cast(bf16x8, __builtin_shufflevector(lo, hi, 0, 1, 2, 3, 4, 5, 6, 7));
}
#define LO16(x) __builtin_shufflevector(x, x, 0, 1, 2, 3, 4, 5, 6, 7, 8, 9, 10, 11, 12, 13, 14, 15)
#define HI16(x) __builtin_shufflevector(x, x, 16, 17, 18, 19, 20, 21, 22, 23, 24, 25, 26, 27, 28, 29, 30, 31)
#define LO8(x) __builtin_bit_cast(bf16x16, __builtin_shufflevector(x, x, 0, 1, 2, 3, 4, 5, 6, 7))
#define HI8(x) __builtin_bit_cast(bf16x16, __builtin_shufflevector(x, x, 8, 9, 10, 11, 12, 13, 14, 15))
#define SMF(a, b, c) __builtin_amdgcn_smfmac_f32_32x32x32_bf16(a, b, c, sel, 0, 0)
#define MF16(a, b, c) __builtin_amdgcn_mfma_f32_32x32x16_bf16(a, b, c, 0, 0, 0)
#define MF8(a, b, c) __builtin_amdgcn_mfma_f32_32x32x8bf16_1k(a, b, c, 0, 0, 0)
#define SBAR() __builtin_amdgcn_sched_barrier(0)
#define PIN(x) asm volatile("" : "+v"(x))
#define EXPS(X, lo, hi) do { _Pragma("unroll") for (int i_ = lo; i_ < hi; ++i_) X[i_] = __builtin_amdgcn_exp2f(X[i_]); PIN(X); SBAR(); } while (0)
#define LDK(p) (*(const LDS_AS(bf16x8)*)(p))
#define LDKX(p) __builtin_bit_cast(s16x4, (u32x2){*(const LDS_AS(unsigned)*)(p), ONE_ZERO})

DI void prep_q(const unsigned* p, int hi, float c, bf16x8& qm, unsigned& qx) {
    const u32x4 a = *(const u32x4_u*)(p + 4 * hi);
    const unsigned e = p[8];
    float s0 = sq2(a[0], 0.f), s1 = sq2(a[1], 0.f);
    s0 = sq2(a[2], s0); s1 = sq2(a[3], s1);
    s0 += s1;
    auto sw = __builtin_amdgcn_permlane32_swap(__float_as_uint(s0), __float_as_uint(s0), false, false);
    const float ss = sq2(e, __uint_as_float(sw[0]) + __uint_as_float(sw[1]));
    const float g = c * inv_norm(ss);
    qm = __builtin_bit_cast(bf16x8, (u32x4){scale2(a[0], g), scale2(a[1], g), scale2(a[2], g), scale2(a[3], g)});
    qx = scale2(e, g);
}
DI void store_o(unsigned* op, const f32x16& o, int hi) {
    auto sw = __builtin_amdgcn_permlane32_swap(__float_as_uint(o[10]), __float_as_uint(o[10]), false, false);
    const float rl = 1.0f / __uint_as_float(sw[0]);
    *(u32x2_u*)(op + 2 * hi) = (u32x2){cvt_pk(o[0] * rl, o[1] * rl), cvt_pk(o[2] * rl, o[3] * rl)};
    *(u32x2_u*)(op + 4 + 2 * hi) = (u32x2){cvt_pk(o[4] * rl, o[5] * rl), cvt_pk(o[6] * rl, o[7] * rl)};
    if (!hi) op[8] = cvt_pk(o[8] * rl, o[9] * rl);
}

__global__ __launch_bounds__(512) void qk_norm_attn(const unsigned* __restrict__ q, const unsigned* __restrict__ k,
                                                    const unsigned* __restrict__ v, const unsigned* __restrict__ t,
                                                    unsigned* __restrict__ out STAMP_ARG) {
    STAMP_AT(0);
    __shared__ __attribute__((aligned(16))) char smem[LDS_BYTES];
    lds_ptr lds = (lds_ptr)smem;
    const int tid = threadIdx.x, lane = tid & 63, r32 = lane & 31, hi = lane >> 5;
    const int wid = __builtin_amdgcn_readfirstlane(tid >> 6);
    const int b = blockIdx.x;
    const int head = (b & 7) + 8 * (b >> 5), qc = (b >> 3) & 3;
    const size_t hbase = (size_t)head * SEQ * 9;
    const int qrow = qc * 512 + wid * 64 + r32;
    const bool is_k = wid < 4;
    const int srow = tid & 255;
    const char* src = (const char*)((is_k ? k : v) + hbase);
    const unsigned soff = srow * 36;

    Row9 raw = load_row(src + ((2 * qc) & 7) * 9216, soff);
    const unsigned tw = t[(head & (NHEADS - 1)) >> 1];
    const float c = __uint_as_float((head & 1) ? (tw & 0xffff0000u) : (tw << 16)) * LOG2E;
    const bool fast = __builtin_fabsf(c) <= SHIFT_SAFE;
    bf16x8 qm0, qm1; unsigned qe0, qe1;
    prep_q(q + hbase + (size_t)qrow * 9, hi, c, qm0, qe0);
    prep_q(q + hbase + (size_t)(qrow + 32) * 9, hi, c, qm1, qe1);
    const unsigned shift_bits = fast ? (cvt_pk(-__builtin_fabsf(c), 0.f) & 0xffffu) : 0u;
    const s16x4 qx = __builtin_bit_cast(s16x4, (u32x2){hi ? qe1 : qe0, shift_bits});

#define EVENT(ch) do { if (is_k) stage_k(lds, (ch) * 256 + srow, raw); else stage_v(lds, (ch) * 256 + srow, raw); \
        if ((ch) < 7) raw = load_row(src + (((ch) + 1 + 2 * qc) & 7) * 9216, soff); \
        __syncthreads(); } while (0)
    PIN(qm0); PIN(qm1); PIN(qe0); PIN(qe1);
    STAMP_AT(1);
    EVENT(0);
    STAMP_AT(2);

    lds_ptr kmp = lds + KM_OFF + lane * 16;
    lds_ptr kxp = lds + KX_OFF + r32 * 4;
    const int i16 = lane & 15, q4 = i16 >> 2, p4 = i16 & 3;
    const int voff = (lane & 16) ? (p4 == 0 ? 128 + q4 * 8 : 160) : q4 * 32 + p4 * 8;
    lds_ptr vp = lds + V_OFF + hi * 2 * VGRP + voff;
    int sel = 0xDDDD;
    PIN(sel); PIN(kmp); PIN(kxp); PIN(vp);
    f32x16 o0, o1, zero;
#pragma unroll
    for (int i = 0; i < 16; ++i) { o0[i] = 0.f; o1[i] = 0.f; zero[i] = 0.f; }

    if (fast) {
        f32x16 a0, a1, b0, b1;
        bf16x8 kf = LDK(kmp), va0, va1, vb0, vb1;
        s16x4 kx = LDKX(kxp);
        f32x32 zero32;
#pragma unroll
        for (int i = 0; i < 32; ++i) zero32[i] = 0.f;
#define QKX(N0, N1) do { f32x32 s_ = __builtin_amdgcn_mfma_f32_32x32x4bf16_1k(kx, qx, zero32, 0, 0, 0); N0 = LO16(s_); N1 = HI16(s_); } while (0)
        QKX(a0, a1);
        a0 = MF16(kf, qm0, a0); a1 = MF16(kf, qm1, a1);
        kf = LDK(kmp + 1024); kx = LDKX(kxp + 128);
        va0 = v_frag(vp); va1 = v_frag(vp + 4 * VGRP);
#define HEAD(C0, N0, N1) QKX(N0, N1); PIN(N0); PIN(N1); SBAR(); kx = LDKX(kxp + KO / 8); SBAR(); EXPS(C0, 0, 10);
#define STEP(C0, C1, N0, N1, VC0, VC1, VN0, VN1, KOFF, VOFF) do { constexpr int KO = KOFF; \
        VN0 = v_frag(vp + VOFF); SBAR(); \
        HEAD(C0, N0, N1) \
        o0 = SMF(VC0, LO8(C0), o0); PIN(o0); SBAR(); \
        VN1 = v_frag(vp + VOFF + 4 * VGRP); SBAR(); \
        EXPS(C0, 10, 16); EXPS(C1, 0, 2); \
        o0 = SMF(VC1, HI8(C0), o0); PIN(o0); SBAR(); \
        EXPS(C1, 2, 6); \
        N0 = MF16(kf, qm0, N0); PIN(N0); SBAR(); \
        EXPS(C1, 6, 10); \
        o1 = SMF(VC0, LO8(C1), o1); PIN(o1); SBAR(); \
        EXPS(C1, 10, 14); \
        N1 = MF16(kf, qm1, N1); PIN(N1); SBAR(); \
        kf = LDK(kmp + KO); SBAR(); \
        EXPS(C1, 14, 16); \
        o1 = SMF(VC1, HI8(C1), o1); PIN(o1); SBAR(); \
    } while (0)
        for (int i = 0; i < 32; ++i) {
            if ((i & 3) == 3 && i < 28) { EVENT((i >> 2) + 1); }
            STEP(a0, a1, b0, b1, va0, va1, vb0, vb1, 2048, 8 * VGRP);
            STEP(b0, b1, a0, a1, vb0, vb1, va0, va1, 3072, 16 * VGRP);
            kmp += 2048; kxp += 256; vp += 16 * VGRP; PIN(kmp); PIN(kxp); PIN(vp);
        }
    } else {
        float m0 = -INFINITY, m1 = -INFINITY;
        for (int i = 0; i < 64; ++i) {
            if ((i & 7) == 0 && i > 0) { EVENT(i >> 3); }
            const bf16x8 kf = LDK(kmp);
            const s16x4 kx = LDKX(kxp);
            const bf16x8 v0 = v_frag(vp), v1 = v_frag(vp + 4 * VGRP);
            f32x32 zero32;
#pragma unroll
            for (int j = 0; j < 32; ++j) zero32[j] = 0.f;
            f32x16 s0, s1; QKX(s0, s1);
            s0 = MF16(kf, qm0, s0); s1 = MF16(kf, qm1, s1);
#define ONLINE(S, M, O) do { float mx = S[0]; _Pragma("unroll") for (int j = 1; j < 16; ++j) mx = __builtin_fmaxf(mx, S[j]); \
                auto sw = __builtin_amdgcn_permlane32_swap(__float_as_uint(mx), __float_as_uint(mx), false, false); \
                const float mn = __builtin_fmaxf(M, __builtin_fmaxf(__uint_as_float(sw[0]), __uint_as_float(sw[1]))); \
                const float alpha = __builtin_amdgcn_exp2f(M - mn); M = mn; \
                _Pragma("unroll") for (int j = 0; j < 16; ++j) { O[j] *= alpha; S[j] = __builtin_amdgcn_exp2f(S[j] - mn); } \
                O = SMF(v0, LO8(S), O); O = SMF(v1, HI8(S), O); } while (0)
            ONLINE(s0, m0, o0);
            ONLINE(s1, m1, o1);
            kmp += 1024; kxp += 128; vp += 8 * VGRP;
        }
        __builtin_amdgcn_s_waitcnt(0x0F70);
    }
    store_o(out + hbase + (size_t)qrow * 9, o0, hi);
    store_o(out + hbase + (size_t)(qrow + 32) * 9, o1, hi);
    STAMP_AT(5);
}

extern "C" void kernel_launch(void* const* d_in, const int* in_sizes, int n_in, void* d_out, int out_size, void* d_ws,
                              size_t ws_size, hipStream_t stream) {
    qk_norm_attn<<<256, 512, 0, stream>>>((const unsigned*)d_in[0], (const unsigned*)d_in[1], (const unsigned*)d_in[2],
                                          (const unsigned*)d_in[3], (unsigned*)d_out STAMP_PASS);
}
```

```cpp
#include <hip/hip_runtime.h>
#include <hip/hip_bf16.h>

typedef __attribute__((ext_vector_type(8))) __bf16 bf16x8;
typedef __attribute__((ext_vector_type(16))) __bf16 bf16x16;
typedef __attribute__((ext_vector_type(2))) __bf16 bf16x2;
typedef __attribute__((ext_vector_type(4))) short s16x4;
typedef __attribute__((ext_vector_type(2))) float f32x2;
typedef __attribute__((ext_vector_type(16))) float f32x16;
typedef __attribute__((ext_vector_type(32))) float f32x32;
typedef __attribute__((ext_vector_type(2))) unsigned u32x2;
typedef __attribute__((ext_vector_type(4))) unsigned u32x4;
typedef __attribute__((ext_vector_type(8))) unsigned u32x8;
typedef __attribute__((ext_vector_type(16))) unsigned u32x16;
typedef __attribute__((address_space(3))) char* lds_ptr;
typedef const __attribute__((address_space(1))) char* g_ptr;
#define G_AS(T) const __attribute__((address_space(1))) T
#define LDS_AS(T) __attribute__((address_space(3))) T

#define DI __device__ __forceinline__
#define STAMP_AT(i)
#define STAMP_ARG
#define STAMP_PASS

#define ABL 0
constexpr int SEQ = 2048, NHEADS = 32;
constexpr int KX_OFF = 256;
constexpr int KM_OFF = KX_OFF + 8192;
constexpr int V_OFF = KM_OFF + 65536;
constexpr int LDS_BYTES = V_OFF + 256 * 320 + 1280;
constexpr float LOG2E = 1.4426950408889634f;
constexpr float SHIFT_SAFE = 40.0f;
constexpr unsigned ONE_ZERO = 0x00003f80u;

typedef u32x4 __attribute__((aligned(4))) u32x4_u;
typedef u32x2 __attribute__((aligned(4))) u32x2_u;
struct Row9 { u32x4 a, b; unsigned c; };

DI Row9 load_row(g_ptr base, unsigned off) { Row9 r; g_ptr p = base + off; r.a = *(G_AS(u32x4_u)*)p; r.b = *(G_AS(u32x4_u)*)(p + 16); r.c = *(G_AS(unsigned)*)(p + 32); return r; }
DI unsigned cvt_pk(float lo, float hi) { f32x2 f = {lo, hi}; return __builtin_bit_cast(unsigned, __builtin_convertvector(f, bf16x2)); }
DI float sq2(unsigned w, float acc) { return __builtin_amdgcn_fdot2_f32_bf16(__builtin_bit_cast(bf16x2, w), __builtin_bit_cast(bf16x2, w), acc, false); }
DI unsigned scale2(unsigned w, float g) { return cvt_pk(__uint_as_float(w << 16) * g, __uint_as_float(w & 0xffff0000u) * g); }
DI float inv_norm(float ss) { return __builtin_amdgcn_rcpf(__builtin_amdgcn_sqrtf(ss) + 1e-6f); }

DI g_ptr uniform_ptr(const void* p) {
    const unsigned long long a = (unsigned long long)p;
    const unsigned lo = __builtin_amdgcn_readfirstlane((unsigned)a), hi = __builtin_amdgcn_readfirstlane((unsigned)(a >> 32));
    return (g_ptr)(((unsigned long long)hi << 32) | lo);
}
DI void stage_k(lds_ptr pm, lds_ptr px, const Row9& r) {
    float s0 = sq2(r.a[0], 0.f), s1 = sq2(r.a[1], 0.f);
    s0 = sq2(r.a[2], s0); s1 = sq2(r.a[3], s1); s0 = sq2(r.b[0], s0); s1 = sq2(r.b[1], s1);
    s0 = sq2(r.b[2], s0); s1 = sq2(r.b[3], s1); s0 = sq2(r.c, s0);
    const float g = inv_norm(s0 + s1);
    *(LDS_AS(u32x4)*)pm = (u32x4){scale2(r.a[0], g), scale2(r.a[1], g), scale2(r.a[2], g), scale2(r.a[3], g)};
    *(LDS_AS(u32x4)*)(pm + 512) = (u32x4){scale2(r.b[0], g), scale2(r.b[1], g), scale2(r.b[2], g), scale2(r.b[3], g)};
    *(LDS_AS(unsigned)*)px = scale2(r.c, g);
}
DI void stage_v(lds_ptr pm, lds_ptr px, unsigned cval, const Row9& r) {
#define W2(w, s_lo, s_hi) *(LDS_AS(unsigned short)*)(pm + (s_lo) * 16) = (unsigned short)(w); *(LDS_AS(unsigned short)*)(pm + (s_hi) * 16) = (unsigned short)((w) >> 16)
    W2(r.a[0], 0, 1); W2(r.a[1], 2, 3); W2(r.a[2], 4, 5); W2(r.a[3], 6, 7);
    W2(r.b[0], 8, 9); W2(r.b[1], 10, 19); W2(r.b[2], 12, 13); W2(r.b[3], 14, 15);
    W2(r.c, 16, 17);
#undef W2
    *(LDS_AS(unsigned)*)px = cval;
}
#define LO16(x) __builtin_shufflevector(x, x, 0, 1, 2, 3, 4, 5, 6, 7, 8, 9, 10, 11, 12, 13, 14, 15)
#define HI16(x) __builtin_shufflevector(x, x, 16, 17, 18, 19, 20, 21, 22, 23, 24, 25, 26, 27, 28, 29, 30, 31)
#define LO8(x) __builtin_bit_cast(bf16x16, __builtin_shufflevector(x, x, 0, 1, 2, 3, 4, 5, 6, 7))
#define HI8(x) __builtin_bit_cast(bf16x16, __builtin_shufflevector(x, x, 8, 9, 10, 11, 12, 13, 14, 15))
#define SMF(a, b, c) __builtin_amdgcn_smfmac_f32_32x32x32_bf16(a, b, c, sel, 0, 0)
#define MF16(a, b, c) __builtin_amdgcn_mfma_f32_32x32x16_bf16(a, b, c, 0, 0, 0)
#define PIN(x) asm volatile("" : "+v"(x))
#define LDB128(p) (*(const LDS_AS(bf16x8)*)(p))

DI void prep_q(const unsigned* p, int hi, float c, u32x4& qm, unsigned& qx) {
    const u32x4 a = *(const u32x4_u*)(p + 4 * hi);
    const unsigned e = p[8];
    float s0 = sq2(a[0], 0.f), s1 = sq2(a[1], 0.f);
    s0 = sq2(a[2], s0); s1 = sq2(a[3], s1);
    s0 += s1;
    auto sw = __builtin_amdgcn_permlane32_swap(__float_as_uint(s0), __float_as_uint(s0), false, false);
    const float ss = sq2(e, __uint_as_float(sw[0]) + __uint_as_float(sw[1]));
    const float g = c * inv_norm(ss);
    qm = (u32x4){scale2(a[0], g), scale2(a[1], g), scale2(a[2], g), scale2(a[3], g)};
    qx = scale2(e, g);
}
DI void store_o(unsigned* op, const f32x16& o, int hi) {
    auto sw = __builtin_amdgcn_permlane32_swap(__float_as_uint(o[10]), __float_as_uint(o[10]), false, false);
    const float rl = 1.0f / __uint_as_float(sw[0]);
    *(u32x2_u*)(op + 2 * hi) = (u32x2){cvt_pk(o[0] * rl, o[1] * rl), cvt_pk(o[2] * rl, o[3] * rl)};
    *(u32x2_u*)(op + 4 + 2 * hi) = (u32x2){cvt_pk(o[4] * rl, o[5] * rl), cvt_pk(o[6] * rl, o[7] * rl)};
    if (!hi) op[8] = cvt_pk(o[8] * rl, o[9] * rl);
}

#define EX(C, i) "v_exp_f32 v[" #C "+" #i "], v[" #C "+" #i "]\n\t"
#define EX2(C, a, b) EX(C, a) EX(C, b)
#define EX3(C, a, b, c) EX(C, a) EX(C, b) EX(C, c)
#define EX5(C, a, b, c, d, e) EX3(C, a, b, c) EX2(C, d, e)
#define PV(O, V, P, i) "v_smfmac_f32_32x32x32_bf16 v[" #O ":" #O "+15], v[" #V ":" #V "+3], v[" #P "+" #i ":" #P "+" #i "+7], v132\n\t"
#define QK(N, i, K, Q) "v_mfma_f32_32x32x16_bf16 v[" #N "+" #i ":" #N "+" #i "+15], v[" #K ":" #K "+3], v[" #Q ":" #Q "+3], v[" #N "+" #i ":" #N "+" #i "+15]\n\t"
#define QX(N, ab) "v_mfma_f32_32x32x4_2b_bf16 v[" #N ":" #N "+31], v[128:129], v[130:131], 0 cbsz:1 abid:" #ab "\n\t"
#define LD128(D, A, off) "ds_read_b128 v[" #D ":" #D "+3], " A " offset:" off "\n\t"
#define WAIT0 "s_waitcnt lgkmcnt(0)\n\t"
#define PRIO(n) "s_setprio " #n "\n\t"
#define STEP(j, W, X, C, N, VC0, VC1, VN0, VN1, KU, KL, ab, KXLOAD) \
    W EX2(C, 0, 1) PV(16, VN1, N, 24) LD128(VN0, "v135", "(" #j "+1)*1280") EX3(C, 2, 3, 4) \
    QX(N, ab) LD128(VN1, "v135", "(" #j "+1)*1280+640") EX5(C, 5, 6, 7, 8, 9) EX3(C, 10, 11, 12) \
    PV(0, VC0, C, 0) LD128(KL, "v133", "(" #j "+2)*1024") EX5(C, 13, 14, 15, 16, 17) \
    PV(0, VC1, C, 8) KXLOAD EX3(C, 18, 19, 20) \
    QK(N, 0, KU, 96) X EX5(C, 21, 22, 23, 24, 25) PV(16, VC0, C, 16) EX3(C, 26, 27, 28) QK(N, 16, KU, 100) EX3(C, 29, 30, 31)
#define STEP_EVEN(j, W, X) STEP(j, W, X, 32, 64, 104, 108, 112, 116, 124, 120, 1, "ds_read_b32 v128, v134 offset:(" #j "+2)*128\n\t")
#define STEP_ODD(j, W, X) STEP(j, W, X, 64, 32, 112, 116, 104, 108, 120, 124, 0, "")

#define SITE(n, KPART, VPART) \
    "s_cbranch_vccz .Lqk_e" #n "_%=\n\ts_cbranch_scc1 .Lqk_v" #n "_%=\n\t" KPART "s_branch .Lqk_e" #n "_%=\n" \
    ".Lqk_v" #n "_%=:\n\t" VPART ".Lqk_e" #n "_%=:\n\t"
#define V_W(r, lo, hi) "ds_write_b16 v146, v" #r " offset:" #lo "*16\n\tds_write_b16_d16_hi v146, v" #r " offset:" #hi "*16\n\t"
#define DOT(d, a, b, c) "v_dot2_f32_bf16 v[" #d "], v[" #a "], v[" #b "], " c "\n\t"
#define K_NORM \
    DOT(149, 136, 136, "0") DOT(150, 137, 137, "0") DOT(149, 138, 138, "v149") DOT(150, 139, 139, "v150") DOT(149, 140, 140, "v149") \
    DOT(150, 141, 141, "v150") DOT(149, 142, 142, "v149") DOT(150, 143, 143, "v150") DOT(149, 144, 144, "v149") \
    "s_nop 2\n\tv_add_f32 v149, v149, v150\n\tv_sqrt_f32 v149, v149\n\ts_nop 0\n\tv_add_f32 v149, 0x358637bd, v149\n\tv_rcp_f32 v149, v149\n\ts_nop 0\n\t" \
    "v_cvt_pk_bf16_f32 v158, v149, 0\n\tv_cvt_pk_bf16_f32 v159, 0, v149\n\t"
#define K_SCALE4(r) \
    DOT(150, r, 158, "0") DOT(154, r, 159, "0") DOT(151, r + 1, 158, "0") DOT(155, r + 1, 159, "0") \
    DOT(152, r + 2, 158, "0") DOT(156, r + 2, 159, "0") DOT(153, r + 3, 158, "0") DOT(157, r + 3, 159, "0") \
    "v_cvt_pk_bf16_f32 v150, v150, v154\n\tv_cvt_pk_bf16_f32 v151, v151, v155\n\ts_nop 0\n\t" \
    "v_cvt_pk_bf16_f32 v152, v152, v156\n\tv_cvt_pk_bf16_f32 v153, v153, v157\n\t"
#define K_SCALE1 DOT(150, 144, 158, "0") DOT(154, 144, 159, "0") "s_nop 2\n\tv_cvt_pk_bf16_f32 v150, v150, v154\n\t"

__global__ __launch_bounds__(512) void qk_norm_attn(const unsigned* __restrict__ q, const unsigned* __restrict__ k,
                                                    const unsigned* __restrict__ v, const unsigned* __restrict__ t,
                                                    unsigned* __restrict__ out STAMP_ARG) {
    STAMP_AT(0);
    __shared__ __attribute__((aligned(16))) char smem[LDS_BYTES];
    lds_ptr lds = (lds_ptr)smem;
    const int tid = threadIdx.x, lane = tid & 63, r32 = lane & 31, hi = lane >> 5;
    const int wid = __builtin_amdgcn_readfirstlane(tid >> 6);
    const int b = blockIdx.x;
    const int head = (b & 7) + 8 * (b >> 5), qc = (b >> 3) & 3;
    const size_t hbase = (size_t)head * SEQ * 9;
    const int qrow = qc * 512 + wid * 64 + r32;
    const bool is_k = wid < 4;
    const int srow = tid & 255;
    g_ptr src = uniform_ptr((is_k ? k : v) + hbase);
    unsigned soff = srow * 36;

    Row9 raw = load_row(src, soff);
    const unsigned tw = t[(head & (NHEADS - 1)) >> 1];
    const float c = __uint_as_float((head & 1) ? (tw & 0xffff0000u) : (tw << 16)) * LOG2E;
    const bool fast = __builtin_fabsf(c) <= SHIFT_SAFE;
    u32x4 qm0, qm1; unsigned qe0, qe1;
    prep_q(q + hbase + (size_t)qrow * 9, hi, c, qm0, qe0);
    prep_q(q + hbase + (size_t)(qrow + 32) * 9, hi, c, qm1, qe1);
    const unsigned shift_bits = fast ? (cvt_pk(-__builtin_fabsf(c), 0.f) & 0xffffu) : 0u;
    u32x2 qx = (u32x2){hi ? qe1 : qe0, shift_bits};

    lds_ptr sm = is_k ? lds + KM_OFF + (srow >> 5) * 1024 + (srow & 31) * 16 : lds + V_OFF + (srow >> 3) * 320 + (srow & 7) * 2;
    lds_ptr sx = is_k ? lds + KX_OFF + srow * 4 : lds + V_OFF + (srow >> 3) * 320 + ((srow & 4) ? 11 * 16 : 18 * 16) + (srow & 3) * 4;
    const unsigned cval = (srow & 4) ? 0u : 0x3f803f80u;
    const int sm_step = is_k ? 8192 : 10240, sx_step = is_k ? 1024 : 10240;
#define STAGE(ch) do { if (is_k) stage_k(sm, sx, raw); else stage_v(sm, sx, cval, raw); \
        sm += sm_step; sx += sx_step; \
        if ((ch) < 7) raw = load_row(src + ((ch) + 1) * 9216, soff); } while (0)
#define EVENT(ch) do { STAGE(ch); __syncthreads(); } while (0)
    PIN(qm0); PIN(qm1); PIN(qx);
    STAMP_AT(1);
    Row9 nxt;
    asm volatile("global_load_dwordx4 v[136:139], v145, %[src]\n\tglobal_load_dwordx4 v[140:143], v145, %[src] offset:16\n\t"
                 "global_load_dword v144, v145, %[src] offset:32"
                 : "=&{v[136:139]}"(nxt.a), "=&{v[140:143]}"(nxt.b), "=&{v144}"(nxt.c), "+v"(raw.a), "+v"(raw.b), "+v"(raw.c)
                 : "{v145}"(soff), [src] "s"(src + 9216) : "memory");
    if (is_k) stage_k(sm, sx, raw); else stage_v(sm, sx, cval, raw);
    sm += sm_step; sx += sx_step;
    __syncthreads();
    STAMP_AT(2);

    const int vslot = r32 == 11 ? 19 : (r32 > 18 ? 11 : r32);
    lds_ptr kmp = lds + KM_OFF + lane * 16;
    lds_ptr kxp = lds + KX_OFF + lane * 4;
    lds_ptr vp = lds + V_OFF + hi * 320 + vslot * 16;
    int sel = 0xDDDD;
    f32x16 o0, o1;
#pragma unroll
    for (int i = 0; i < 16; ++i) { o0[i] = 0.f; o1[i] = 0.f; }

    if (fast) {
        int tmp;
        f32x32 sa, sb; u32x16 vf; u32x8 kf; u32x2 kx = (u32x2){0u, ONE_ZERO};
        kmp -= 2048; kxp -= 256; vp -= 2560;
        asm volatile(
            "ds_read_b32 v128, v134 offset:256\n\t" LD128(120, "v133", "2048") LD128(124, "v133", "3072")
            LD128(104, "v135", "2560") LD128(108, "v135", "3200")
            "v_mov_b32 v116, 0\n\tv_mov_b32 v117, 0\n\tv_mov_b32 v118, 0\n\tv_mov_b32 v119, 0\n\t"
            "v_mov_b32 v88, 0\n\tv_mov_b32 v89, 0\n\tv_mov_b32 v90, 0\n\tv_mov_b32 v91, 0\n\t"
            "v_mov_b32 v92, 0\n\tv_mov_b32 v93, 0\n\tv_mov_b32 v94, 0\n\tv_mov_b32 v95, 0\n\t"
            "s_waitcnt lgkmcnt(4)\n\t" QX(32, 0) "s_nop 15\n\ts_nop 1\n\ts_waitcnt lgkmcnt(0)\n\t" QK(32, 0, 120, 96) QK(32, 16, 120, 100) "s_nop 11"
            : "=&{v[32:63]}"(sa), "=&{v[64:95]}"(sb), "=&{v[104:119]}"(vf), "=&{v[120:127]}"(kf), "+{v[128:129]}"(kx)
            : "{v[96:99]}"(qm0), "{v[100:103]}"(qm1), "{v[130:131]}"(qx), "{v133}"(kmp), "{v134}"(kxp), "{v135}"(vp) : "memory");
        unsigned long long nsrc = (unsigned long long)(src + 2 * 9216);
        int it = 0;
        asm volatile(
            "s_branch .Lqk_mid_%=\n"
            ".Lqk_top_%=:\n\t"
            PRIO(3) STEP_EVEN(0, WAIT0, "") STEP_ODD(1, WAIT0, "")
            "s_cmp_eq_u32 %[it], 8\n\ts_cbranch_scc1 .Lqk_end_%=\n"
            ".Lqk_mid_%=:\n\t"
            "s_cmp_lt_u32 %[it], 7\n\ts_cselect_b64 vcc, -1, 0\n\ts_cmp_gt_u32 %[wid], 3\n\t"
            PRIO(2)
            STEP_EVEN(2, WAIT0, SITE(a, "s_waitcnt vmcnt(0)\n\t" K_NORM, "s_waitcnt vmcnt(2)\n\t" V_W(136, 0, 1) V_W(137, 2, 3) "ds_write_b32 v147, v148\n\t"))
            STEP_ODD(3, WAIT0, SITE(b, K_SCALE4(136) "ds_write_b128 v146, v[150:153]\n\t", V_W(138, 4, 5) V_W(139, 6, 7)) "global_load_dwordx4 v[136:139], v145, %[src]\n\t")
            PRIO(1)
            STEP_EVEN(4, WAIT0, SITE(c, "", "s_waitcnt vmcnt(2)\n\t" V_W(140, 8, 9) V_W(141, 10, 19)))
            STEP_ODD(5, WAIT0, SITE(d, K_SCALE4(140) "ds_write_b128 v146, v[150:153] offset:512\n\t", V_W(142, 12, 13) V_W(143, 14, 15)) "global_load_dwordx4 v[140:143], v145, %[src] offset:16\n\t")
            PRIO(0)
            STEP_EVEN(6, WAIT0, SITE(e, K_SCALE1 "ds_write_b32 v147, v150\n\t", "s_waitcnt vmcnt(2)\n\t" V_W(144, 16, 17)) "global_load_dword v144, v145, %[src] offset:32\n\t")
            STEP_ODD(7, WAIT0, "v_add_u32 v146, %[smst], v146\n\tv_add_u32 v147, %[sxst], v147\n\t")
            "v_add_u32 v133, 0x2000, v133\n\tv_add_u32 v134, 0x400, v134\n\tv_add_u32 v135, 0x2800, v135\n\t"
            "s_add_u32 %[it], %[it], 1\n\ts_cmp_lt_u32 %[it], 6\n\ts_cselect_b32 %[tmp], 0x2400, 0\n\t"
            "s_add_u32 s20, s20, %[tmp]\n\ts_addc_u32 s21, s21, 0\n\t"
            "s_waitcnt lgkmcnt(0)\n\ts_barrier\n\ts_branch .Lqk_top_%=\n"
            ".Lqk_end_%=:\n\t"
            "s_waitcnt vmcnt(0) lgkmcnt(0)"
            : "+{v[0:15]}"(o0), "+{v[16:31]}"(o1), "+{v[32:63]}"(sa), "+{v[64:95]}"(sb), "+{v[104:119]}"(vf), "+{v[120:127]}"(kf),
              "+{v[128:129]}"(kx), "+{v133}"(kmp), "+{v134}"(kxp), "+{v135}"(vp),
              "+{v[136:139]}"(nxt.a), "+{v[140:143]}"(nxt.b), "+{v144}"(nxt.c), "+{v146}"(sm), "+{v147}"(sx),
              [it] "+s"(it), [src] "+{s[20:21]}"(nsrc), [tmp] "=&s"(tmp)
            : "{v[96:99]}"(qm0), "{v[100:103]}"(qm1), "{v[130:131]}"(qx), "{v132}"(sel), "{v145}"(soff), "{v148}"(cval),
              [wid] "s"(wid), [smst] "s"(sm_step), [sxst] "s"(sx_step)
            : "memory", "scc", "vcc", "v149", "v150", "v151", "v152", "v153", "v154", "v155", "v156", "v157", "v158", "v159");
        asm volatile("s_nop 1\n\t" PV(16, 116, 64, 24) "s_nop 11"
                     : "+{v[0:15]}"(o0), "+{v[16:31]}"(o1) : "{v[64:95]}"(sb), "{v[104:119]}"(vf), "{v132}"(sel));
    } else {
        float m0 = -INFINITY, m1 = -INFINITY;
        asm volatile("s_waitcnt vmcnt(0)" :: "{v[136:139]}"(nxt.a), "{v[140:143]}"(nxt.b), "{v144}"(nxt.c) : "memory");
        raw = load_row(src + 9216, soff);
        PIN(sel);
        kxp = lds + KX_OFF + r32 * 4;
        const bf16x8 q0 = __builtin_bit_cast(bf16x8, qm0), q1 = __builtin_bit_cast(bf16x8, qm1);
        const s16x4 qxs = __builtin_bit_cast(s16x4, qx);
#pragma unroll 1
        for (int i = 0; i < 64; ++i) {
            if ((i & 7) == 0 && i > 0) EVENT(i >> 3);
            const bf16x8 kf = LDB128(kmp), v0 = LDB128(vp), v1 = LDB128(vp + 640);
            const s16x4 kx = __builtin_bit_cast(s16x4, (u32x2){*(const LDS_AS(unsigned)*)kxp, ONE_ZERO});
            f32x32 zero32;
#pragma unroll
            for (int j = 0; j < 32; ++j) zero32[j] = 0.f;
            const f32x32 sx32 = __builtin_amdgcn_mfma_f32_32x32x4bf16_1k(kx, qxs, zero32, 0, 0, 0);
            f32x16 s0 = MF16(kf, q0, LO16(sx32)), s1 = MF16(kf, q1, HI16(sx32));
#define ONLINE(S, M, O) do { float mx = S[0]; _Pragma("unroll") for (int j = 1; j < 16; ++j) mx = __builtin_fmaxf(mx, S[j]); \
                auto sw = __builtin_amdgcn_permlane32_swap(__float_as_uint(mx), __float_as_uint(mx), false, false); \
                const float mn = __builtin_fmaxf(M, __builtin_fmaxf(__uint_as_float(sw[0]), __uint_as_float(sw[1]))); \
                const float alpha = __builtin_amdgcn_exp2f(M - mn); M = mn; \
                _Pragma("unroll") for (int j = 0; j < 16; ++j) { O[j] *= alpha; S[j] = __builtin_amdgcn_exp2f(S[j] - mn); } \
                O = SMF(v0, LO8(S), O); O = SMF(v1, HI8(S), O); } while (0)
            ONLINE(s0, m0, o0);
            ONLINE(s1, m1, o1);
            kmp += 1024; kxp += 128; vp += 1280;
        }
        __builtin_amdgcn_s_waitcnt(0x0F70);
    }
    store_o(out + hbase + (size_t)qrow * 9, o0, hi);
    store_o(out + hbase + (size_t)(qrow + 32) * 9, o1, hi);
    STAMP_AT(5);
}

extern "C" void kernel_launch(void* const* d_in, const int* in_sizes, int n_in, void* d_out, int out_size, void* d_ws,
                              size_t ws_size, hipStream_t stream) {
    qk_norm_attn<<<256, 512, 0, stream>>>((const unsigned*)d_in[0], (const unsigned*)d_in[1], (const unsigned*)d_in[2],
                                          (const unsigned*)d_in[3], (unsigned*)d_out STAMP_PASS);
}
```

```cpp
#include <hip/hip_runtime.h>
#include <hip/hip_bf16.h>

typedef __attribute__((ext_vector_type(8))) __bf16 bf16x8;
typedef __attribute__((ext_vector_type(16))) __bf16 bf16x16;
typedef __attribute__((ext_vector_type(2))) __bf16 bf16x2;
typedef __attribute__((ext_vector_type(4))) short s16x4;
typedef __attribute__((ext_vector_type(2))) float f32x2;
typedef __attribute__((ext_vector_type(16))) float f32x16;
typedef __attribute__((ext_vector_type(32))) float f32x32;
typedef __attribute__((ext_vector_type(2))) unsigned u32x2;
typedef __attribute__((ext_vector_type(4))) unsigned u32x4;
typedef __attribute__((ext_vector_type(8))) unsigned u32x8;
typedef __attribute__((ext_vector_type(16))) unsigned u32x16;
typedef __attribute__((address_space(3))) char* lds_ptr;
typedef const __attribute__((address_space(1))) char* g_ptr;
#define G_AS(T) const __attribute__((address_space(1))) T
#define LDS_AS(T) __attribute__((address_space(3))) T

#define DI __device__ __forceinline__
#define STAMP_AT(i)
#define STAMP_ARG
#define STAMP_PASS

#define ABL 0
constexpr int SEQ = 2048, NHEADS = 32;
constexpr int KX_OFF = 256;
constexpr int KM_OFF = KX_OFF + 8192;
constexpr int V_OFF = KM_OFF + 65536;
constexpr int LDS_BYTES = V_OFF + 256 * 320 + 1280;
constexpr float LOG2E = 1.4426950408889634f;
constexpr float SHIFT_SAFE = 40.0f;
constexpr unsigned ONE_ZERO = 0x00003f80u;

typedef u32x4 __attribute__((aligned(4))) u32x4_u;
typedef u32x2 __attribute__((aligned(4))) u32x2_u;
struct Row9 { u32x4 a, b; unsigned c; };

DI Row9 load_row(g_ptr base, unsigned off) { Row9 r; g_ptr p = base + off; r.a = *(G_AS(u32x4_u)*)p; r.b = *(G_AS(u32x4_u)*)(p + 16); r.c = *(G_AS(unsigned)*)(p + 32); return r; }
DI unsigned cvt_pk(float lo, float hi) { f32x2 f = {lo, hi}; return __builtin_bit_cast(unsigned, __builtin_convertvector(f, bf16x2)); }
DI float sq2(unsigned w, float acc) { return __builtin_amdgcn_fdot2_f32_bf16(__builtin_bit_cast(bf16x2, w), __builtin_bit_cast(bf16x2, w), acc, false); }
DI unsigned scale2(unsigned w, float g) { return cvt_pk(__uint_as_float(w << 16) * g, __uint_as_float(w & 0xffff0000u) * g); }
DI float inv_norm(float ss) { return __builtin_amdgcn_rcpf(__builtin_amdgcn_sqrtf(ss) + 1e-6f); }

DI g_ptr uniform_ptr(const void* p) {
    const unsigned long long a = (unsigned long long)p;
    const unsigned lo = __builtin_amdgcn_readfirstlane((unsigned)a), hi = __builtin_amdgcn_readfirstlane((unsigned)(a >> 32));
    return (g_ptr)(((unsigned long long)hi << 32) | lo);
}
DI void stage_k(lds_ptr pm, lds_ptr px, const Row9& r) {
    float s0 = sq2(r.a[0], 0.f), s1 = sq2(r.a[1], 0.f);
    s0 = sq2(r.a[2], s0); s1 = sq2(r.a[3], s1); s0 = sq2(r.b[0], s0); s1 = sq2(r.b[1], s1);
    s0 = sq2(r.b[2], s0); s1 = sq2(r.b[3], s1); s0 = sq2(r.c, s0);
    const float g = inv_norm(s0 + s1);
    *(LDS_AS(u32x4)*)pm = (u32x4){scale2(r.a[0], g), scale2(r.a[1], g), scale2(r.a[2], g), scale2(r.a[3], g)};
    *(LDS_AS(u32x4)*)(pm + 512) = (u32x4){scale2(r.b[0], g), scale2(r.b[1], g), scale2(r.b[2], g), scale2(r.b[3], g)};
    *(LDS_AS(unsigned)*)px = scale2(r.c, g);
}
DI void stage_v(lds_ptr pm, lds_ptr px, unsigned cval, const Row9& r) {
#define W2(w, s_lo, s_hi) *(LDS_AS(unsigned short)*)(pm + (s_lo) * 16) = (unsigned short)(w); *(LDS_AS(unsigned short)*)(pm + (s_hi) * 16) = (unsigned short)((w) >> 16)
    W2(r.a[0], 0, 1); W2(r.a[1], 2, 3); W2(r.a[2], 4, 5); W2(r.a[3], 6, 7);
    W2(r.b[0], 8, 9); W2(r.b[1], 10, 19); W2(r.b[2], 12, 13); W2(r.b[3], 14, 15);
    W2(r.c, 16, 17);
#undef W2
    *(LDS_AS(unsigned)*)px = cval;
}
#define LO16(x) __builtin_shufflevector(x, x, 0, 1, 2, 3, 4, 5, 6, 7, 8, 9, 10, 11, 12, 13, 14, 15)
#define HI16(x) __builtin_shufflevector(x, x, 16, 17, 18, 19, 20, 21, 22, 23, 24, 25, 26, 27, 28, 29, 30, 31)
#define LO8(x) __builtin_bit_cast(bf16x16, __builtin_shufflevector(x, x, 0, 1, 2, 3, 4, 5, 6, 7))
#define HI8(x) __builtin_bit_cast(bf16x16, __builtin_shufflevector(x, x, 8, 9, 10, 11, 12, 13, 14, 15))
#define SMF(a, b, c) __builtin_amdgcn_smfmac_f32_32x32x32_bf16(a, b, c, sel, 0, 0)
#define MF16(a, b, c) __builtin_amdgcn_mfma_f32_32x32x16_bf16(a, b, c, 0, 0, 0)
#define PIN(x) asm volatile("" : "+v"(x))
#define LDB128(p) (*(const LDS_AS(bf16x8)*)(p))

DI void prep_q(const unsigned* p, int hi, float c, u32x4& qm, unsigned& qx) {
    const u32x4 a = *(const u32x4_u*)(p + 4 * hi);
    const unsigned e = p[8];
    float s0 = sq2(a[0], 0.f), s1 = sq2(a[1], 0.f);
    s0 = sq2(a[2], s0); s1 = sq2(a[3], s1);
    s0 += s1;
    auto sw = __builtin_amdgcn_permlane32_swap(__float_as_uint(s0), __float_as_uint(s0), false, false);
    const float ss = sq2(e, __uint_as_float(sw[0]) + __uint_as_float(sw[1]));
    const float g = c * inv_norm(ss);
    qm = (u32x4){scale2(a[0], g), scale2(a[1], g), scale2(a[2], g), scale2(a[3], g)};
    qx = scale2(e, g);
}
DI void put_o(lds_ptr p, const f32x16& o, int hi) {
    auto sw = __builtin_amdgcn_permlane32_swap(__float_as_uint(o[10]), __float_as_uint(o[10]), false, false);
    const float rl = __builtin_amdgcn_rcpf(__uint_as_float(sw[0]));
    LDS_AS(unsigned)* w = (LDS_AS(unsigned)*)(p + 8 * hi);
    w[0] = cvt_pk(o[0] * rl, o[1] * rl); w[1] = cvt_pk(o[2] * rl, o[3] * rl);
    w[4] = cvt_pk(o[4] * rl, o[5] * rl); w[5] = cvt_pk(o[6] * rl, o[7] * rl);
    if (!hi) w[8] = cvt_pk(o[8] * rl, o[9] * rl);
}
#define STMOD "sc1"
DI void store16(g_ptr base, unsigned off, u32x4 x) { asm volatile("global_store_dwordx4 %0, %1, %2 " STMOD "\n\ts_nop 1" :: "v"(off), "v"(x), "s"(base) : "memory"); }

#define EX(C, i) "v_exp_f32 v[" #C "+" #i "], v[" #C "+" #i "]\n\t"
#define EX2(C, a, b) EX(C, a) EX(C, b)
#define EX3(C, a, b, c) EX(C, a) EX(C, b) EX(C, c)
#define EX5(C, a, b, c, d, e) EX3(C, a, b, c) EX2(C, d, e)
#define PV(O, V, P, i) "v_smfmac_f32_32x32x32_bf16 v[" #O ":" #O "+15], v[" #V ":" #V "+3], v[" #P "+" #i ":" #P "+" #i "+7], v132\n\t"
#define QK(N, i, K, Q) "v_mfma_f32_32x32x16_bf16 v[" #N "+" #i ":" #N "+" #i "+15], v[" #K ":" #K "+3], v[" #Q ":" #Q "+3], v[" #N "+" #i ":" #N "+" #i "+15]\n\t"
#define QX(N, ab) "v_mfma_f32_32x32x4_2b_bf16 v[" #N ":" #N "+31], v[128:129], v[130:131], 0 cbsz:1 abid:" #ab "\n\t"
#define LD128(D, A, off) "ds_read_b128 v[" #D ":" #D "+3], " A " offset:" off "\n\t"
#define WAIT0 "s_waitcnt lgkmcnt(0)\n\t"
#define PRIO(n) "s_setprio " #n "\n\t"
#define STEP(j, W, X, C, N, VC0, VC1, VN0, VN1, KU, KL, ab, KXLOAD) \
    W EX2(C, 0, 1) PV(16, VN1, N, 24) LD128(VN0, "v135", "(" #j "+1)*1280") EX3(C, 2, 3, 4) \
    QX(N, ab) LD128(VN1, "v135", "(" #j "+1)*1280+640") EX5(C, 5, 6, 7, 8, 9) EX3(C, 10, 11, 12) \
    PV(0, VC0, C, 0) LD128(KL, "v133", "(" #j "+2)*1024") EX5(C, 13, 14, 15, 16, 17) \
    PV(0, VC1, C, 8) KXLOAD EX3(C, 18, 19, 20) \
    QK(N, 0, KU, 96) X EX5(C, 21, 22, 23, 24, 25) PV(16, VC0, C, 16) EX3(C, 26, 27, 28) QK(N, 16, KU, 100) EX3(C, 29, 30, 31)
#define STEP_EVEN(j, W, X) STEP(j, W, X, 32, 64, 104, 108, 112, 116, 124, 120, 1, "ds_read_b32 v128, v134 offset:(" #j "+2)*128\n\t")
#define STEP_ODD(j, W, X) STEP(j, W, X, 64, 32, 112, 116, 104, 108, 120, 124, 0, "")

#define SITE(n, KPART, VPART) \
    "s_cbranch_vccz .Lqk_e" #n "_%=\n\ts_cbranch_scc1 .Lqk_v" #n "_%=\n\t" KPART "s_branch .Lqk_e" #n "_%=\n" \
    ".Lqk_v" #n "_%=:\n\t" VPART ".Lqk_e" #n "_%=:\n\t"
#define V_W(r, lo, hi) "ds_write_b16 v146, v" #r " offset:" #lo "*16\n\tds_write_b16_d16_hi v146, v" #r " offset:" #hi "*16\n\t"
#define DOT(d, a, b, c) "v_dot2_f32_bf16 v[" #d "], v[" #a "], v[" #b "], " c "\n\t"
#define K_NORM \
    DOT(149, 136, 136, "0") DOT(150, 137, 137, "0") DOT(149, 138, 138, "v149") DOT(150, 139, 139, "v150") DOT(149, 140, 140, "v149") \
    DOT(150, 141, 141, "v150") DOT(149, 142, 142, "v149") DOT(150, 143, 143, "v150") DOT(149, 144, 144, "v149") \
    "s_nop 2\n\tv_add_f32 v149, v149, v150\n\tv_sqrt_f32 v149, v149\n\ts_nop 0\n\tv_add_f32 v149, 0x358637bd, v149\n\tv_rcp_f32 v149, v149\n\ts_nop 0\n\t" \
    "v_cvt_pk_bf16_f32 v158, v149, 0\n\tv_cvt_pk_bf16_f32 v159, 0, v149\n\t"
#define K_SCALE4(r) \
    DOT(150, r, 158, "0") DOT(154, r, 159, "0") DOT(151, r + 1, 158, "0") DOT(155, r + 1, 159, "0") \
    DOT(152, r + 2, 158, "0") DOT(156, r + 2, 159, "0") DOT(153, r + 3, 158, "0") DOT(157, r + 3, 159, "0") \
    "v_cvt_pk_bf16_f32 v150, v150, v154\n\tv_cvt_pk_bf16_f32 v151, v151, v155\n\ts_nop 0\n\t" \
    "v_cvt_pk_bf16_f32 v152, v152, v156\n\tv_cvt_pk_bf16_f32 v153, v153, v157\n\t"
#define K_SCALE1 DOT(150, 144, 158, "0") DOT(154, 144, 159, "0") "s_nop 2\n\tv_cvt_pk_bf16_f32 v150, v150, v154\n\t"

__global__ __launch_bounds__(512) void qk_norm_attn(const unsigned* __restrict__ q, const unsigned* __restrict__ k,
                                                    const unsigned* __restrict__ v, const unsigned* __restrict__ t,
                                                    unsigned* __restrict__ out STAMP_ARG) {
    STAMP_AT(0);
    __shared__ __attribute__((aligned(16))) char smem[LDS_BYTES];
    lds_ptr lds = (lds_ptr)smem;
    const int tid = threadIdx.x, lane = tid & 63, r32 = lane & 31, hi = lane >> 5;
    const int wid = __builtin_amdgcn_readfirstlane(tid >> 6);
    const int b = blockIdx.x;
    const int head = (b & 7) + 8 * (b >> 5), qc = (b >> 3) & 3;
    const size_t hbase = (size_t)head * SEQ * 9;
    const int qrow = qc * 512 + wid * 64 + r32;
    const bool is_k = wid < 4;
    const int srow = tid & 255;
    g_ptr src = uniform_ptr((is_k ? k : v) + hbase);
    unsigned soff = srow * 36;

    Row9 raw = load_row(src, soff);
    const unsigned tw = t[(head & (NHEADS - 1)) >> 1];
    const float c = __uint_as_float((head & 1) ? (tw & 0xffff0000u) : (tw << 16)) * LOG2E;
    const bool fast = __builtin_fabsf(c) <= SHIFT_SAFE;
    u32x4 qm0, qm1; unsigned qe0, qe1;
    prep_q(q + hbase + (size_t)qrow * 9, hi, c, qm0, qe0);
    prep_q(q + hbase + (size_t)(qrow + 32) * 9, hi, c, qm1, qe1);
    const unsigned shift_bits = fast ? (cvt_pk(-__builtin_fabsf(c), 0.f) & 0xffffu) : 0u;
    u32x2 qx = (u32x2){hi ? qe1 : qe0, shift_bits};

    lds_ptr sm = is_k ? lds + KM_OFF + (srow >> 5) * 1024 + (srow & 31) * 16 : lds + V_OFF + (srow >> 3) * 320 + (srow & 7) * 2;
    lds_ptr sx = is_k ? lds + KX_OFF + srow * 4 : lds + V_OFF + (srow >> 3) * 320 + ((srow & 4) ? 11 * 16 : 18 * 16) + (srow & 3) * 4;
    const unsigned cval = (srow & 4) ? 0u : 0x3f803f80u;
    const int sm_step = is_k ? 8192 : 10240, sx_step = is_k ? 1024 : 10240;
#define STAGE(ch) do { if (is_k) stage_k(sm, sx, raw); else stage_v(sm, sx, cval, raw); \
        sm += sm_step; sx += sx_step; \
        if ((ch) < 7) raw = load_row(src + ((ch) + 1) * 9216, soff); } while (0)
#define EVENT(ch) do { STAGE(ch); __syncthreads(); } while (0)
    PIN(qm0); PIN(qm1); PIN(qx);
    STAMP_AT(1);
    Row9 nxt;
    asm volatile("global_load_dwordx4 v[136:139], v145, %[src]\n\tglobal_load_dwordx4 v[140:143], v145, %[src] offset:16\n\t"
                 "global_load_dword v144, v145, %[src] offset:32"
                 : "=&{v[136:139]}"(nxt.a), "=&{v[140:143]}"(nxt.b), "=&{v144}"(nxt.c), "+v"(raw.a), "+v"(raw.b), "+v"(raw.c)
                 : "{v145}"(soff), [src] "s"(src + 9216) : "memory");
    if (is_k) stage_k(sm, sx, raw); else stage_v(sm, sx, cval, raw);
    sm += sm_step; sx += sx_step;
    __syncthreads();
    STAMP_AT(2);

    const int vslot = r32 == 11 ? 19 : (r32 > 18 ? 11 : r32);
    lds_ptr kmp = lds + KM_OFF + lane * 16;
    lds_ptr kxp = lds + KX_OFF + lane * 4;
    lds_ptr vp = lds + V_OFF + hi * 320 + vslot * 16;
    int sel = 0xDDDD;
    f32x16 o0, o1;
#pragma unroll
    for (int i = 0; i < 16; ++i) { o0[i] = 0.f; o1[i] = 0.f; }

    if (fast) {
        int tmp;
        f32x32 sa, sb; u32x16 vf; u32x8 kf; u32x2 kx = (u32x2){0u, ONE_ZERO};
        kmp -= 2048; kxp -= 256; vp -= 2560;
        asm volatile(
            "ds_read_b32 v128, v134 offset:256\n\t" LD128(120, "v133", "2048") LD128(124, "v133", "3072")
            LD128(104, "v135", "2560") LD128(108, "v135", "3200")
            "v_mov_b32 v116, 0\n\tv_mov_b32 v117, 0\n\tv_mov_b32 v118, 0\n\tv_mov_b32 v119, 0\n\t"
            "v_mov_b32 v88, 0\n\tv_mov_b32 v89, 0\n\tv_mov_b32 v90, 0\n\tv_mov_b32 v91, 0\n\t"
            "v_mov_b32 v92, 0\n\tv_mov_b32 v93, 0\n\tv_mov_b32 v94, 0\n\tv_mov_b32 v95, 0\n\t"
            "s_waitcnt lgkmcnt(4)\n\t" QX(32, 0) "s_nop 15\n\ts_nop 1\n\ts_waitcnt lgkmcnt(0)\n\t" QK(32, 0, 120, 96) QK(32, 16, 120, 100) "s_nop 11"
            : "=&{v[32:63]}"(sa), "=&{v[64:95]}"(sb), "=&{v[104:119]}"(vf), "=&{v[120:127]}"(kf), "+{v[128:129]}"(kx)
            : "{v[96:99]}"(qm0), "{v[100:103]}"(qm1), "{v[130:131]}"(qx), "{v133}"(kmp), "{v134}"(kxp), "{v135}"(vp) : "memory");
        unsigned long long nsrc = (unsigned long long)(src + 2 * 9216);
        int it = 0;
        asm volatile(
            "s_branch .Lqk_mid_%=\n"
            ".Lqk_top_%=:\n\t"
            PRIO(3) STEP_EVEN(0, WAIT0, "") STEP_ODD(1, WAIT0, "")
            "s_cmp_eq_u32 %[it], 8\n\ts_cbranch_scc1 .Lqk_end_%=\n"
            ".Lqk_mid_%=:\n\t"
            "s_cmp_lt_u32 %[it], 7\n\ts_cselect_b64 vcc, -1, 0\n\ts_cmp_gt_u32 %[wid], 3\n\t"
            PRIO(2)
            STEP_EVEN(2, WAIT0, SITE(a, "s_waitcnt vmcnt(0)\n\t" K_NORM, "s_waitcnt vmcnt(2)\n\t" V_W(136, 0, 1) V_W(137, 2, 3) "ds_write_b32 v147, v148\n\t"))
            STEP_ODD(3, WAIT0, SITE(b, K_SCALE4(136) "ds_write_b128 v146, v[150:153]\n\t", V_W(138, 4, 5) V_W(139, 6, 7)) "global_load_dwordx4 v[136:139], v145, %[src]\n\t")
            PRIO(1)
            STEP_EVEN(4, WAIT0, SITE(c, "", "s_waitcnt vmcnt(2)\n\t" V_W(140, 8, 9) V_W(141, 10, 19)))
            STEP_ODD(5, WAIT0, SITE(d, K_SCALE4(140) "ds_write_b128 v146, v[150:153] offset:512\n\t", V_W(142, 12, 13) V_W(143, 14, 15)) "global_load_dwordx4 v[140:143], v145, %[src] offset:16\n\t")
            PRIO(0)
            STEP_EVEN(6, WAIT0, SITE(e, K_SCALE1 "ds_write_b32 v147, v150\n\t", "s_waitcnt vmcnt(2)\n\t" V_W(144, 16, 17)) "global_load_dword v144, v145, %[src] offset:32\n\t")
            STEP_ODD(7, WAIT0, "v_add_u32 v146, %[smst], v146\n\tv_add_u32 v147, %[sxst], v147\n\t")
            "v_add_u32 v133, 0x2000, v133\n\tv_add_u32 v134, 0x400, v134\n\tv_add_u32 v135, 0x2800, v135\n\t"
            "s_add_u32 %[it], %[it], 1\n\ts_cmp_lt_u32 %[it], 6\n\ts_cselect_b32 %[tmp], 0x2400, 0\n\t"
            "s_add_u32 s20, s20, %[tmp]\n\ts_addc_u32 s21, s21, 0\n\t"
            "s_waitcnt lgkmcnt(0)\n\ts_barrier\n\ts_branch .Lqk_top_%=\n"
            ".Lqk_end_%=:\n\t"
            "s_waitcnt vmcnt(0) lgkmcnt(0)"
            : "+{v[0:15]}"(o0), "+{v[16:31]}"(o1), "+{v[32:63]}"(sa), "+{v[64:95]}"(sb), "+{v[104:119]}"(vf), "+{v[120:127]}"(kf),
              "+{v[128:129]}"(kx), "+{v133}"(kmp), "+{v134}"(kxp), "+{v135}"(vp),
              "+{v[136:139]}"(nxt.a), "+{v[140:143]}"(nxt.b), "+{v144}"(nxt.c), "+{v146}"(sm), "+{v147}"(sx),
              [it] "+s"(it), [src] "+{s[20:21]}"(nsrc), [tmp] "=&s"(tmp)
            : "{v[96:99]}"(qm0), "{v[100:103]}"(qm1), "{v[130:131]}"(qx), "{v132}"(sel), "{v145}"(soff), "{v148}"(cval),
              [wid] "s"(wid), [smst] "s"(sm_step), [sxst] "s"(sx_step)
            : "memory", "scc", "vcc", "v149", "v150", "v151", "v152", "v153", "v154", "v155", "v156", "v157", "v158", "v159");
        asm volatile("s_nop 1\n\t" PV(16, 116, 64, 24) "s_nop 11"
                     : "+{v[0:15]}"(o0), "+{v[16:31]}"(o1) : "{v[64:95]}"(sb), "{v[104:119]}"(vf), "{v132}"(sel));
    } else {
        float m0 = -INFINITY, m1 = -INFINITY;
        asm volatile("s_waitcnt vmcnt(0)" :: "{v[136:139]}"(nxt.a), "{v[140:143]}"(nxt.b), "{v144}"(nxt.c) : "memory");
        raw = load_row(src + 9216, soff);
        PIN(sel);
        kxp = lds + KX_OFF + r32 * 4;
        const bf16x8 q0 = __builtin_bit_cast(bf16x8, qm0), q1 = __builtin_bit_cast(bf16x8, qm1);
        const s16x4 qxs = __builtin_bit_cast(s16x4, qx);
#pragma unroll 1
        for (int i = 0; i < 64; ++i) {
            if ((i & 7) == 0 && i > 0) EVENT(i >> 3);
            const bf16x8 kf = LDB128(kmp), v0 = LDB128(vp), v1 = LDB128(vp + 640);
            const s16x4 kx = __builtin_bit_cast(s16x4, (u32x2){*(const LDS_AS(unsigned)*)kxp, ONE_ZERO});
            f32x32 zero32;
#pragma unroll
            for (int j = 0; j < 32; ++j) zero32[j] = 0.f;
            const f32x32 sx32 = __builtin_amdgcn_mfma_f32_32x32x4bf16_1k(kx, qxs, zero32, 0, 0, 0);
            f32x16 s0 = MF16(kf, q0, LO16(sx32)), s1 = MF16(kf, q1, HI16(sx32));
#define ONLINE(S, M, O) do { float mx = S[0]; _Pragma("unroll") for (int j = 1; j < 16; ++j) mx = __builtin_fmaxf(mx, S[j]); \
                auto sw = __builtin_amdgcn_permlane32_swap(__float_as_uint(mx), __float_as_uint(mx), false, false); \
                const float mn = __builtin_fmaxf(M, __builtin_fmaxf(__uint_as_float(sw[0]), __uint_as_float(sw[1]))); \
                const float alpha = __builtin_amdgcn_exp2f(M - mn); M = mn; \
                _Pragma("unroll") for (int j = 0; j < 16; ++j) { O[j] *= alpha; S[j] = __builtin_amdgcn_exp2f(S[j] - mn); } \
                O = SMF(v0, LO8(S), O); O = SMF(v1, HI8(S), O); } while (0)
            ONLINE(s0, m0, o0);
            ONLINE(s1, m1, o1);
            kmp += 1024; kxp += 128; vp += 1280;
        }
        __builtin_amdgcn_s_waitcnt(0x0F70);
    }
    {
        lds_ptr patch = lds + KM_OFF + wid * 2304;
        put_o(patch + r32 * 36, o0, hi);
        put_o(patch + (r32 + 32) * 36, o1, hi);
        __builtin_amdgcn_fence(__ATOMIC_RELEASE, "wavefront");
        __builtin_amdgcn_wave_barrier();
        g_ptr dst = uniform_ptr(out + hbase + (size_t)(qc * 512 + wid * 64) * 9);
        const u32x4 x0 = *(LDS_AS(u32x4)*)(patch + lane * 16), x1 = *(LDS_AS(u32x4)*)(patch + 1024 + lane * 16);
        store16(dst, lane * 16, x0);
        store16(dst, 1024 + lane * 16, x1);
        if (lane < 16) store16(dst, 2048 + lane * 16, *(LDS_AS(u32x4)*)(patch + 2048 + lane * 16));
    }
    STAMP_AT(5);
}

extern "C" void kernel_launch(void* const* d_in, const int* in_sizes, int n_in, void* d_out, int out_size, void* d_ws,
                              size_t ws_size, hipStream_t stream) {
    qk_norm_attn<<<256, 512, 0, stream>>>((const unsigned*)d_in[0], (const unsigned*)d_in[1], (const unsigned*)d_in[2],
                                          (const unsigned*)d_in[3], (unsigned*)d_out STAMP_PASS);
}
```

```cpp
#include <hip/hip_runtime.h>
#include <hip/hip_bf16.h>

typedef __attribute__((ext_vector_type(8))) __bf16 bf16x8;
typedef __attribute__((ext_vector_type(16))) __bf16 bf16x16;
typedef __attribute__((ext_vector_type(2))) __bf16 bf16x2;
typedef __attribute__((ext_vector_type(4))) short s16x4;
typedef __attribute__((ext_vector_type(2))) float f32x2;
typedef __attribute__((ext_vector_type(16))) float f32x16;
typedef __attribute__((ext_vector_type(32))) float f32x32;
typedef __attribute__((ext_vector_type(2))) unsigned u32x2;
typedef __attribute__((ext_vector_type(4))) unsigned u32x4;
typedef __attribute__((ext_vector_type(8))) unsigned u32x8;
typedef __attribute__((ext_vector_type(16))) unsigned u32x16;
typedef __attribute__((address_space(3))) char* lds_ptr;
typedef const __attribute__((address_space(1))) char* g_ptr;
#define G_AS(T) const __attribute__((address_space(1))) T
#define LDS_AS(T) __attribute__((address_space(3))) T

#define DI __device__ __forceinline__
#define STAMP_AT(i)
#define STAMP_DECL
#define STAMP_ARG
#define STAMP_PASS

#define PSTAMP(i)
#define MSTAMP(i) STAMP_AT(i)
#define ABL 0
constexpr int SEQ = 2048, NHEADS = 32;
constexpr int KX_OFF = 256;
constexpr int KM_OFF = KX_OFF + 8192;
constexpr int V_OFF = KM_OFF + 65536;
constexpr int LDS_BYTES = V_OFF + 256 * 320 + 1280;
constexpr float LOG2E = 1.4426950408889634f;
constexpr float SHIFT_SAFE = 40.0f;
constexpr unsigned ONE_ZERO = 0x00003f80u;

typedef u32x4 __attribute__((aligned(4))) u32x4_u;
typedef u32x2 __attribute__((aligned(4))) u32x2_u;
struct Row9 { u32x4 a, b; unsigned c; };

DI Row9 load_row(g_ptr base, unsigned off) { Row9 r; g_ptr p = base + off; r.a = *(G_AS(u32x4_u)*)p; r.b = *(G_AS(u32x4_u)*)(p + 16); r.c = *(G_AS(unsigned)*)(p + 32); return r; }
DI unsigned cvt_pk(float lo, float hi) { f32x2 f = {lo, hi}; return __builtin_bit_cast(unsigned, __builtin_convertvector(f, bf16x2)); }
DI float sq2(unsigned w, float acc) { return __builtin_amdgcn_fdot2_f32_bf16(__builtin_bit_cast(bf16x2, w), __builtin_bit_cast(bf16x2, w), acc, false); }
DI unsigned scale2(unsigned w, float g) { return cvt_pk(__uint_as_float(w << 16) * g, __uint_as_float(w & 0xffff0000u) * g); }
DI float inv_norm(float ss) { return __builtin_amdgcn_rcpf(__builtin_amdgcn_sqrtf(ss) + 1e-6f); }

DI g_ptr uniform_ptr(const void* p) {
    const unsigned long long a = (unsigned long long)p;
    const unsigned lo = __builtin_amdgcn_readfirstlane((unsigned)a), hi = __builtin_amdgcn_readfirstlane((unsigned)(a >> 32));
    return (g_ptr)(((unsigned long long)hi << 32) | lo);
}
DI void stage_k(lds_ptr pm, lds_ptr px, const Row9& r) {
    float s0 = sq2(r.a[0], 0.f), s1 = sq2(r.a[1], 0.f);
    s0 = sq2(r.a[2], s0); s1 = sq2(r.a[3], s1); s0 = sq2(r.b[0], s0); s1 = sq2(r.b[1], s1);
    s0 = sq2(r.b[2], s0); s1 = sq2(r.b[3], s1); s0 = sq2(r.c, s0);
    const float g = inv_norm(s0 + s1);
    *(LDS_AS(u32x4)*)pm = (u32x4){scale2(r.a[0], g), scale2(r.a[1], g), scale2(r.a[2], g), scale2(r.a[3], g)};
    *(LDS_AS(u32x4)*)(pm + 512) = (u32x4){scale2(r.b[0], g), scale2(r.b[1], g), scale2(r.b[2], g), scale2(r.b[3], g)};
    *(LDS_AS(unsigned)*)px = scale2(r.c, g);
}
DI void stage_v(lds_ptr pm, lds_ptr px, unsigned cval, const Row9& r) {
#define W2(w, s_lo, s_hi) *(LDS_AS(unsigned short)*)(pm + (s_lo) * 16) = (unsigned short)(w); *(LDS_AS(unsigned short)*)(pm + (s_hi) * 16) = (unsigned short)((w) >> 16)
    W2(r.a[0], 0, 1); W2(r.a[1], 2, 3); W2(r.a[2], 4, 5); W2(r.a[3], 6, 7);
    W2(r.b[0], 8, 9); W2(r.b[1], 10, 19); W2(r.b[2], 12, 13); W2(r.b[3], 14, 15);
    W2(r.c, 16, 17);
#undef W2
    *(LDS_AS(unsigned)*)px = cval;
}
#define LO16(x) __builtin_shufflevector(x, x, 0, 1, 2, 3, 4, 5, 6, 7, 8, 9, 10, 11, 12, 13, 14, 15)
#define HI16(x) __builtin_shufflevector(x, x, 16, 17, 18, 19, 20, 21, 22, 23, 24, 25, 26, 27, 28, 29, 30, 31)
#define LO8(x) __builtin_bit_cast(bf16x16, __builtin_shufflevector(x, x, 0, 1, 2, 3, 4, 5, 6, 7))
#define HI8(x) __builtin_bit_cast(bf16x16, __builtin_shufflevector(x, x, 8, 9, 10, 11, 12, 13, 14, 15))
#define SMF(a, b, c) __builtin_amdgcn_smfmac_f32_32x32x32_bf16(a, b, c, sel, 0, 0)
#define MF16(a, b, c) __builtin_amdgcn_mfma_f32_32x32x16_bf16(a, b, c, 0, 0, 0)
#define PIN(x) asm volatile("" : "+v"(x))
#define LDB128(p) (*(const LDS_AS(bf16x8)*)(p))

DI void prep_q(const u32x4 a, const unsigned e, float c, u32x4& qm, unsigned& qx) {
    float s0 = sq2(a[0], 0.f), s1 = sq2(a[1], 0.f);
    s0 = sq2(a[2], s0); s1 = sq2(a[3], s1);
    s0 += s1;
    auto sw = __builtin_amdgcn_permlane32_swap(__float_as_uint(s0), __float_as_uint(s0), false, false);
    const float ss = sq2(e, __uint_as_float(sw[0]) + __uint_as_float(sw[1]));
    const float g = c * inv_norm(ss);
    qm = (u32x4){scale2(a[0], g), scale2(a[1], g), scale2(a[2], g), scale2(a[3], g)};
    qx = scale2(e, g);
}
DI void put_o(lds_ptr p, const f32x16& o, int hi) {
    auto sw = __builtin_amdgcn_permlane32_swap(__float_as_uint(o[10]), __float_as_uint(o[10]), false, false);
    const float rl = __builtin_amdgcn_rcpf(__uint_as_float(sw[0]));
    LDS_AS(unsigned)* w = (LDS_AS(unsigned)*)(p + 8 * hi);
    w[0] = cvt_pk(o[0] * rl, o[1] * rl); w[1] = cvt_pk(o[2] * rl, o[3] * rl);
    w[4] = cvt_pk(o[4] * rl, o[5] * rl); w[5] = cvt_pk(o[6] * rl, o[7] * rl);
    if (!hi) w[8] = cvt_pk(o[8] * rl, o[9] * rl);
}
#define STMOD "sc1"
DI void store16(g_ptr base, unsigned off, u32x4 x) { asm volatile("global_store_dwordx4 %0, %1, %2 " STMOD "\n\ts_nop 1" :: "v"(off), "v"(x), "s"(base) : "memory"); }

#define EX(C, i) "v_exp_f32 v[" #C "+" #i "], v[" #C "+" #i "]\n\t"
#define EXL(C, i) "v_exp_f32_e64 v[" #C "+" #i "], v[" #C "+" #i "]\n\t"
#define EX2(C, a, b) EX(C, a) EX(C, b)
#define EX3(C, a, b, c) EX2(C, a, b) EXL(C, c)
#define EX5(C, a, b, c, d, e) EX2(C, a, b) EX3(C, c, d, e)
#define EX8(C, a, b, c, d, e, f, g, h) EX2(C, a, b) EX2(C, c, d) EX2(C, e, f) EX2(C, g, h)
#define PV(O, V, P, i) "v_smfmac_f32_32x32x32_bf16 v[" #O ":" #O "+15], v[" #V ":" #V "+3], v[" #P "+" #i ":" #P "+" #i "+7], v132\n\t"
#define QK(N, i, K, Q) "v_mfma_f32_32x32x16_bf16 v[" #N "+" #i ":" #N "+" #i "+15], v[" #K ":" #K "+3], v[" #Q ":" #Q "+3], v[" #N "+" #i ":" #N "+" #i "+15]\n\t"
#define QX(N, ab) "v_mfma_f32_32x32x4_2b_bf16 v[" #N ":" #N "+31], v[128:129], v[130:131], 0 cbsz:1 abid:" #ab "\n\t"
#define LD128(D, A, off) "ds_read_b128 v[" #D ":" #D "+3], " A " offset:" off "\n\t"
#define WAIT0 "s_waitcnt lgkmcnt(0)\n\t"
#define PLANK 3, 3, 3, 3, 2, 2, 1, 0
#define PLANV PLANK
#define HD(p, w, C) "s_setprio " #p "\n\ts_waitcnt " w "lgkmcnt(0)\n\t" EX2(C, 0, 1)
#define PRIO(n) "s_setprio " #n "\n\t"
#define STEP(j, P, W, C, N, VC0, VC1, VN0, VN1, KU, KL, ab, H0, H1, H2, H3, H4) \
    HD(P, W, C) PV(16, VN1, N, 24) LD128(VN0, "v135", "(" #j "+1)*1280") EX3(C, 2, 3, 4) \
    QX(N, ab) LD128(VN1, "v135", "(" #j "+1)*1280+640") EX8(C, 5, 6, 7, 8, 9, 10, 11, 12) \
    H0 PV(0, VC0, C, 0) LD128(KL, "v133", "(" #j "+2)*1024") EX5(C, 13, 14, 15, 16, 17) \
    PV(0, VC1, C, 8) H1 EX3(C, 18, 19, 20) \
    QK(N, 0, KU, 96) H2 EX5(C, 21, 22, 23, 24, 25) PV(16, VC0, C, 16) H3 EX3(C, 26, 27, 28) QK(N, 16, KU, 100) H4 EX3(C, 29, 30, 31)
#define STEP_EVEN(j, P, W, H2, H3, H4) STEP(j, P, W, 32, 64, 104, 108, 112, 116, 124, 120, 1, "", "ds_read_b32 v128, v134 offset:(" #j "+2)*128\n\t", H2, H3, H4)
#define STEP_ODD(j, P, W, H1, H2, H3, H4) STEP(j, P, W, 64, 32, 112, 116, 104, 108, 120, 124, 0, "", H1, H2, H3, H4)

#define V_W(r, lo, hi) "ds_write_b16 v146, v" #r " offset:" #lo "*16\n\tds_write_b16_d16_hi v146, v" #r " offset:" #hi "*16\n\t"
#define UNP(d, r) "v_mfma_f32_4x4x4_16b_bf16 v[" #d ":" #d "+3], v[172:173], v[" #r ":" #r "+1], 0\n\t"
#define SQ(acc, x) "v_fmac_f32 v" #acc ", v[" #x "], v[" #x "]\n\t"
#define SQ4(x) SQ(150, x) SQ(151, x + 1) SQ(150, x + 2) SQ(151, x + 3)
#define K_UNPACK UNP(152, 136) UNP(156, 138) UNP(160, 140) UNP(164, 142) UNP(168, 144) \
    "s_nop 0\n\tv_mul_f32 v150, v152, v152\n\tv_mul_f32 v151, v153, v153\n\t" SQ(150, 154) SQ(151, 155) SQ4(156) SQ4(160) SQ4(164) SQ(150, 168) SQ(151, 169) \
    "v_add_f32 v150, v150, v151\n\tv_sqrt_f32 v150, v150\n\ts_nop 0\n\tv_add_f32 v150, 0x358637bd, v150\n\tv_rcp_f32 v150, v150\n\t"
#define SC2(d, x) "v_mul_f32 v[" #x "], v150, v[" #x "]\n\tv_mul_f32 v[" #x "+1], v150, v[" #x "+1]\n\tv_cvt_pk_bf16_f32 v[" #d "], v[" #x "], v[" #x "+1]\n\t"
#define K_SCALE8(x) SC2(x, x) SC2(x + 1, x + 2) SC2(x + 2, x + 4) SC2(x + 3, x + 6)

#define PAD 0
#define STR2(x) #x
#define STR(x) STR2(x)
#define PADN STR(PAD)
#define KMODE 3
#define LATE 0
#define DOT(d, a, b, c) "v_dot2_f32_bf16 v[" #d "], v[" #a "], v[" #b "], " c "\n\t"
#define K_P1 DOT(150, 136, 136, "0") DOT(151, 137, 137, "0") DOT(150, 138, 138, "v150") DOT(151, 139, 139, "v151") DOT(150, 140, 140, "v150") \
    DOT(151, 141, 141, "v151") DOT(150, 142, 142, "v150") DOT(151, 143, 143, "v151") DOT(150, 144, 144, "v150") \
    UNP(152, 136) UNP(156, 138) UNP(160, 140) "v_add_f32 v150, v150, v151\n\tv_sqrt_f32 v150, v150\n\t" UNP(164, 142) \
    "v_add_f32 v150, 0x358637bd, v150\n\tv_rcp_f32_e64 v150, v150\n\t" UNP(168, 144)
#define PKM(x) "v_pk_mul_f32 v[" #x ":" #x "+1], v[150:151], v[" #x ":" #x "+1] op_sel_hi:[0,1]\n\t"
#define CVT(d, x) "v_cvt_pk_bf16_f32 v[" #d "], v[" #x "], v[" #x "+1]\n\t"
#define K_PK8(x) PKM(x) PKM(x + 2) PKM(x + 4) PKM(x + 6) CVT(x, x) CVT(x + 1, x + 2) CVT(x + 2, x + 4) CVT(x + 3, x + 6)
#define K_P2 K_PK8(152) "ds_write_b128 v146, v[152:155]\n\t"
#define K_P3 K_PK8(160) "ds_write_b128 v146, v[160:163] offset:512\n\t"
#define K_P4 PKM(168) CVT(168, 168) "ds_write_b32 v147, v168\n\t"
#define K_P5 ""

#define NOP "s_nop 0\n\t"
#define SITE(n, PART) "s_cbranch_scc1 .Lqk_s" #n "_%=\n\t" NOP PART ".Lqk_s" #n "_%=:\n\t"
#define WL(r, s) "ds_write_b16 v146, v" #r " offset:" #s "*16\n\t"
#define WH(r, s) "ds_write_b16_d16_hi v146, v" #r " offset:" #s "*16\n\t"
#define LDA "global_load_dwordx4 v[136:139], v149, s[20:21]\n\t"
#define LDB "global_load_dwordx4 v[140:143], v149, s[20:21] offset:16\n\t"
#define LDC "global_load_dword v144, v149, s[20:21] offset:32\n\t"
#define FLAG "ds_write_b32 v181, v179\n\t"
#define BODY_TOP(r, p0, p1) \
    ".p2align 6\n\t.fill " PADN ", 4, 0xbf800000\n" \
    ".Lqk_top_" #r "_%=:\n\t" \
    STEP(0, p0, "", 32, 64, 104, 108, 112, 116, 124, 120, 1, \
         "v_cmp_lt_u32 vcc, v180, v179\n\ts_cbranch_vccnz .Lqk_spin_" #r "_%=\n.Lqk_go_" #r "_%=:\n\t", "ds_read_b32 v128, v134 offset:2*128\n\t", "", "", "") \
    STEP_ODD(1, p1, "", "", "v_add_u32_e64 v179, 1, v179\n\t", "", "") \
    "s_cmp_eq_u32 %[it], 8\n\ts_cbranch_scc1 .Lqk_end_%=\n" \
    ".Lqk_mid_" #r "_%=:\n\t" \
    "s_cmp_eq_u32 %[it], 7\n\ts_nop 0\n\t"
#define BODY_END(r, p0, p7) \
    STEP_ODD(7, p7, "", "s_cmp_eq_u32 %[it], 6\n\ts_cselect_b32 %[tmp], -7, 1\n\t", "ds_read_b32 v180, v182\n\tv_mad_i32_i24 v146, %[tmp], v177, v146\n\t", \
             "v_mad_i32_i24 v147, %[tmp], v178, v147\n\t", "v_add_u32 v133, 0x2000, v133\n\t") \
    "v_add_u32 v134, 0x400, v134\n\tv_add_u32 v135, 0x2800, v135\n\t" \
    "s_add_u32 %[it], %[it], 1\n\ts_cmp_lt_u32 %[it], 6\n\ts_cselect_b32 %[tmp], 0x2400, 0\n\t" \
    "s_add_u32 s20, s20, %[tmp]\n\ts_addc_u32 s21, s21, 0\n\t" \
    "s_branch .Lqk_top_" #r "_%=\n" \
    ".Lqk_spin_" #r "_%=:\n\ts_setprio 0\n\tds_read_b32 v180, v182\n\ts_waitcnt lgkmcnt(0)\n\tv_cmp_lt_u32 vcc, v180, v179\n\t" \
    "s_cbranch_vccnz .Lqk_spin_" #r "_%=\n\ts_setprio " #p0 "\n\ts_branch .Lqk_go_" #r "_%=\n\t"
#define BODY_K_(p0, p1, p2, p3, p4, p5, p6, p7) BODY_TOP(k, p0, p1) \
    STEP_EVEN(2, p2, "vmcnt(0) ", SITE(a, K_P1), "", LDA) \
    STEP_ODD(3, p3, "", LDB, SITE(b, K_PK8(152)), "ds_write_b128 v146, v[152:155]\n\t", LDC) \
    STEP_EVEN(4, p4, "", SITE(c, K_PK8(160)), "ds_write_b128 v146, v[160:163] offset:512\n\t", "") \
    STEP_ODD(5, p5, "", "", SITE(d, PKM(168) CVT(168, 168)), "ds_write_b32 v147, v168\n\t", FLAG) \
    STEP_EVEN(6, p6, "", "", "", "") BODY_END(k, p0, p7)
#define BODY_V_(p0, p1, p2, p3, p4, p5, p6, p7) BODY_TOP(v, p0, p1) \
    STEP_EVEN(2, p2, "vmcnt(2) ", WL(136, 0), WH(136, 1) WL(137, 2), WH(137, 3)) \
    STEP_ODD(3, p3, "", WL(138, 4), WH(138, 5), WL(139, 6) WH(139, 7), LDA) \
    STEP_EVEN(4, p4, "vmcnt(2) ", WL(140, 8), WH(140, 9) WL(141, 10), WH(141, 19)) \
    STEP_ODD(5, p5, "", WL(142, 12), WH(142, 13), WL(143, 14) WH(143, 15), LDB) \
    STEP_EVEN(6, p6, "vmcnt(2) ", WL(144, 16), WH(144, 17) "ds_write_b32 v147, v148\n\t", FLAG LDC) BODY_END(v, p0, p7)

#define APPLY(M, ...) M(__VA_ARGS__)
#define BODY_K APPLY(BODY_K_, PLANK)
#define BODY_V APPLY(BODY_V_, PLANV)

#define OMUL(O, i) "v_mul_f32 v[" #O "+" #i "], v[" #O "+10], v[" #O "+" #i "]\n\t"
#define OCVT(O, d, i) "v_cvt_pk_bf16_f32 v[" #O "+" #d "], v[" #O "+" #i "], v[" #O "+" #i "+1]\n\t"
#define OUT_TILE(O, A) \
    "v_mov_b32 v[" #O "+11], v[" #O "+10]\n\ts_nop 1\n\tv_permlane32_swap_b32 v[" #O "+10], v[" #O "+11]\n\tv_rcp_f32 v[" #O "+10], v[" #O "+10]\n\ts_nop 0\n\t" \
    OMUL(O, 0) OMUL(O, 1) OMUL(O, 2) OMUL(O, 3) OMUL(O, 4) OMUL(O, 5) OMUL(O, 6) OMUL(O, 7) OMUL(O, 8) OMUL(O, 9) \
    OCVT(O, 0, 0) OCVT(O, 1, 2) OCVT(O, 2, 4) OCVT(O, 3, 6) OCVT(O, 4, 8) \
    "ds_write2_b32 v" #A ", v[" #O "], v[" #O "+1] offset1:1\n\tds_write2_b32 v" #A ", v[" #O "+2], v[" #O "+3] offset0:4 offset1:5\n\t" \
    "s_mov_b32 exec_hi, 0\n\tds_write_b32 v" #A ", v[" #O "+4] offset:32\n\ts_mov_b32 exec_hi, -1\n\t"
#define ASM_TAIL ""

__global__ __launch_bounds__(512) void qk_norm_attn(const unsigned* __restrict__ q, const unsigned* __restrict__ k,
                                                    const unsigned* __restrict__ v, const unsigned* __restrict__ t,
                                                    unsigned* __restrict__ out STAMP_ARG) {
    STAMP_DECL
    STAMP_AT(0);
    __shared__ __attribute__((aligned(16))) char smem[LDS_BYTES];
    lds_ptr lds = (lds_ptr)smem;
    const int tid = threadIdx.x, lane = tid & 63, r32 = lane & 31, hi = lane >> 5;
    const int wid = __builtin_amdgcn_readfirstlane(tid >> 6);
    const int b = blockIdx.x;
    const int head = (b & 7) + 8 * (b >> 5), qc = (b >> 3) & 3;
    const size_t hbase = (size_t)head * SEQ * 9;
    const int qrow = qc * 512 + wid * 64 + r32;
    const bool is_k = wid < 4;
    const int srow = tid & 255;
    g_ptr src = uniform_ptr((is_k ? k : v) + hbase);
    g_ptr qsrc = uniform_ptr(q + hbase);
    g_ptr dst = uniform_ptr(out + hbase + (size_t)(qc * 512 + wid * 64) * 9);
    const unsigned tw = t[(head & (NHEADS - 1)) >> 1];
    unsigned soff = srow * 36;
    const unsigned qoff = qrow * 36, qoffh = qoff + 16 * hi;
    Row9 raw, nxt; u32x4 qa0, qa1; unsigned qb0, qb1;
#define LDROW(a, b, c, off, base) "global_load_dwordx4 " a ", " off ", " base "\n\tglobal_load_dwordx4 " b ", " off ", " base " offset:16\n\tglobal_load_dword " c ", " off ", " base " offset:32\n\t"
    asm volatile("global_load_dwordx4 %3, %[qh], %[qs]\n\tglobal_load_dword %4, %[qo], %[qs] offset:32\n\t"
                 "global_load_dwordx4 %5, %[qh], %[qs] offset:1152\n\tglobal_load_dword %6, %[qo], %[qs] offset:1184\n\t"
                 LDROW("%0", "%1", "%2", "%[so]", "%[src]")
                 : "=&v"(raw.a), "=&v"(raw.b), "=&v"(raw.c), "=&v"(qa0), "=&v"(qb0), "=&v"(qa1), "=&v"(qb1)
                 : [so] "v"(soff), [src] "s"(src), [qh] "v"(qoffh), [qo] "v"(qoff), [qs] "s"(qsrc) : "memory");
    PSTAMP(1);
    const int sm_step = is_k ? 8192 : 10240, sx_step = is_k ? 1024 : 10240;
    const int vrow = V_OFF + (srow >> 3) * 320;
    lds_ptr sm = lds + (is_k ? KM_OFF + (srow >> 5) * 1024 + (srow & 31) * 16 : vrow + (srow & 7) * 2);
    lds_ptr sx = lds + (is_k ? KX_OFF + srow * 4 : vrow + ((srow & 4) ? 11 * 16 : 18 * 16) + (srow & 3) * 4);
    const unsigned cval = (srow & 4) ? 0u : 0x3f803f80u;
    const float c = __uint_as_float((head & 1) ? (tw & 0xffff0000u) : (tw << 16)) * LOG2E;
    const bool fast = __builtin_fabsf(c) <= SHIFT_SAFE;
    const unsigned shift_bits = fast ? (cvt_pk(-__builtin_fabsf(c), 0.f) & 0xffffu) : 0u;
    f32x16 o0, o1;
#pragma unroll
    for (int i = 0; i < 16; ++i) { o0[i] = 0.f; o1[i] = 0.f; }
    asm volatile("" : "+{v[0:15]}"(o0), "+{v[16:31]}"(o1), "+s"(dst));
    u32x4 qm0, qm1; unsigned qe0, qe1;
    asm volatile("s_waitcnt vmcnt(5)" : "+v"(qa0), "+v"(qb0));
    PSTAMP(2);
    prep_q(qa0, qb0, c, qm0, qe0);
    asm volatile("s_waitcnt vmcnt(3)" : "+v"(qa1), "+v"(qb1));
    prep_q(qa1, qb1, c, qm1, qe1);
    u32x2 qx = (u32x2){hi ? qe1 : qe0, shift_bits};
    PIN(qm0); PIN(qm1); PIN(qx);
    MSTAMP(1);
    asm volatile("s_waitcnt vmcnt(0)\n\t" LDROW("v[136:139]", "v[140:143]", "v144", "v149", "%[src]")
                 : "=&{v[136:139]}"(nxt.a), "=&{v[140:143]}"(nxt.b), "=&{v144}"(nxt.c), "+v"(raw.a), "+v"(raw.b), "+v"(raw.c)
                 : "{v149}"(soff), [src] "s"(src + 9216) : "memory");
    PSTAMP(3);
    if (is_k) stage_k(sm, sx, raw); else stage_v(sm, sx, cval, raw);
    sm += sm_step; sx += sx_step;
    *(LDS_AS(int)*)(lds + wid * 4) = 0;
    PSTAMP(4);
    __syncthreads();
    MSTAMP(2);

    const int vslot = r32 == 11 ? 19 : (r32 > 18 ? 11 : r32);
    lds_ptr kmp = lds + KM_OFF + lane * 16;
    lds_ptr kxp = lds + KX_OFF + lane * 4;
    lds_ptr vp = lds + V_OFF + hi * 320 + vslot * 16;
    int sel = 0xDDDD;
    lds_ptr patch = lds + V_OFF + 20480 + wid * 2304;

    if (fast) {
        int tmp;
        f32x32 sa, sb; u32x16 vf; u32x8 kf; u32x2 kx = (u32x2){0u, ONE_ZERO};
        kmp -= 2048; kxp -= 256; vp -= 2560;
        asm volatile(
            "ds_read_b32 v128, v134 offset:256\n\t" LD128(120, "v133", "2048") LD128(124, "v133", "3072")
            LD128(104, "v135", "2560") LD128(108, "v135", "3200")
            "v_mov_b32 v116, 0\n\tv_mov_b32 v117, 0\n\tv_mov_b32 v118, 0\n\tv_mov_b32 v119, 0\n\t"
            "v_mov_b32 v88, 0\n\tv_mov_b32 v89, 0\n\tv_mov_b32 v90, 0\n\tv_mov_b32 v91, 0\n\t"
            "v_mov_b32 v92, 0\n\tv_mov_b32 v93, 0\n\tv_mov_b32 v94, 0\n\tv_mov_b32 v95, 0\n\t"
            "s_waitcnt lgkmcnt(4)\n\t" QX(32, 0) "s_nop 15\n\ts_nop 1\n\ts_waitcnt lgkmcnt(0)\n\t" QK(32, 0, 120, 96) QK(32, 16, 120, 100) "s_nop 11"
            : "=&{v[32:63]}"(sa), "=&{v[64:95]}"(sb), "=&{v[104:119]}"(vf), "=&{v[120:127]}"(kf), "+{v[128:129]}"(kx)
            : "{v[96:99]}"(qm0), "{v[100:103]}"(qm1), "{v[130:131]}"(qx), "{v133}"(kmp), "{v134}"(kxp), "{v135}"(vp) : "memory");
        unsigned long long nsrc = (unsigned long long)(src + 2 * 9216);
        int it = 0, token = 1;
        lds_ptr orow = patch + r32 * 36 + 8 * hi;
        const u32x2 ident = (u32x2){(lane & 2) ? 0u : (0x3f80u << (16 * (lane & 1))), (lane & 2) ? (0x3f80u << (16 * (lane & 1))) : 0u};
        asm volatile(
            "s_cmp_gt_u32 %[wid], 3\n\ts_cbranch_scc1 .Lqk_mid_v_%=\n\ts_branch .Lqk_mid_k_%=\n\t"
            BODY_K BODY_V
            ".p2align 5\n"
            ".Lqk_end_%=:\n\t"
            "s_nop 1\n\t" PV(16, 116, 64, 24)
            OUT_TILE(0, 174)
            "v_add_u32 v174, 1152, v174\n\t"
            OUT_TILE(16, 174)
            "s_waitcnt lgkmcnt(0)\n\t"
            "ds_read_b128 v[32:35], v175\n\tds_read_b128 v[36:39], v175 offset:1024\n\tds_read_b128 v[40:43], v175 offset:2048\n\t"
            "s_waitcnt lgkmcnt(2)\n\tglobal_store_dwordx4 v176, v[32:35], %[dst] " STMOD "\n\t"
            "s_waitcnt lgkmcnt(1)\n\tglobal_store_dwordx4 v176, v[36:39], %[dst] offset:1024 " STMOD "\n\t"
            "s_mov_b64 exec, 0xffff\n\t"
            "s_waitcnt lgkmcnt(0)\n\tglobal_store_dwordx4 v176, v[40:43], %[dst] offset:2048 " STMOD "\n\t"
            "s_mov_b64 exec, -1\n\t" ASM_TAIL
            : "+{v[0:15]}"(o0), "+{v[16:31]}"(o1), "+{v[32:63]}"(sa), "+{v[64:95]}"(sb), "+{v[104:119]}"(vf), "+{v[120:127]}"(kf),
              "+{v[128:129]}"(kx), "+{v133}"(kmp), "+{v134}"(kxp), "+{v135}"(vp),
              "+{v[136:139]}"(nxt.a), "+{v[140:143]}"(nxt.b), "+{v144}"(nxt.c), "+{v146}"(sm), "+{v147}"(sx), "+{v174}"(orow), "+{v179}"(token),
              [it] "+s"(it), "+{s[20:21]}"(nsrc), [tmp] "=&s"(tmp)
            : "{v[96:99]}"(qm0), "{v[100:103]}"(qm1), "{v[130:131]}"(qx), "{v132}"(sel), "{v145}"(0), "{v148}"(cval), "{v149}"(soff), "{v[172:173]}"(ident), "{v175}"(patch + lane * 16), "{v176}"(lane * 16), [dst] "s"(dst),
              [wid] "s"(wid), "{v177}"(sm_step), "{v178}"(sx_step), "{v181}"(lds + wid * 4), "{v182}"(lds + (lane & 7) * 4)
            : "memory", "scc", "vcc", "v180", "v150", "v151", "v152", "v153", "v154", "v155", "v156", "v157", "v158", "v159", "v160", "v161", "v162", "v163", "v164", "v165", "v166", "v167", "v168", "v169", "v170", "v171");
    } else {
        float m0 = -INFINITY, m1 = -INFINITY;
        asm volatile("s_waitcnt vmcnt(0)" :: "{v[136:139]}"(nxt.a), "{v[140:143]}"(nxt.b), "{v144}"(nxt.c) : "memory");
        PIN(sel);
        kxp = lds + KX_OFF + r32 * 4;
        const bf16x8 q0 = __builtin_bit_cast(bf16x8, qm0), q1 = __builtin_bit_cast(bf16x8, qm1);
        const s16x4 qxs = __builtin_bit_cast(s16x4, qx);
#pragma unroll 1
        for (int i = 0; i < 64; ++i) {
            if ((i & 7) == 0 && i > 0) {
                raw = load_row(src + (i >> 3) * 9216, soff);
                if (is_k) stage_k(sm, sx, raw); else stage_v(sm, sx, cval, raw);
                sm += sm_step; sx += sx_step;
                __syncthreads();
            }
            const bf16x8 kf = LDB128(kmp), v0 = LDB128(vp), v1 = LDB128(vp + 640);
            const s16x4 kx = __builtin_bit_cast(s16x4, (u32x2){*(const LDS_AS(unsigned)*)kxp, ONE_ZERO});
            f32x32 zero32;
#pragma unroll
            for (int j = 0; j < 32; ++j) zero32[j] = 0.f;
            const f32x32 sx32 = __builtin_amdgcn_mfma_f32_32x32x4bf16_1k(kx, qxs, zero32, 0, 0, 0);
            f32x16 s0 = MF16(kf, q0, LO16(sx32)), s1 = MF16(kf, q1, HI16(sx32));
#define ONLINE(S, M, O) do { float mx = S[0]; _Pragma("unroll") for (int j = 1; j < 16; ++j) mx = __builtin_fmaxf(mx, S[j]); \
                auto sw = __builtin_amdgcn_permlane32_swap(__float_as_uint(mx), __float_as_uint(mx), false, false); \
                const float mn = __builtin_fmaxf(M, __builtin_fmaxf(__uint_as_float(sw[0]), __uint_as_float(sw[1]))); \
                const float alpha = __builtin_amdgcn_exp2f(M - mn); M = mn; \
                _Pragma("unroll") for (int j = 0; j < 16; ++j) { O[j] *= alpha; S[j] = __builtin_amdgcn_exp2f(S[j] - mn); } \
                O = SMF(v0, LO8(S), O); O = SMF(v1, HI8(S), O); } while (0)
            ONLINE(s0, m0, o0);
            ONLINE(s1, m1, o1);
            kmp += 1024; kxp += 128; vp += 1280;
        }
    {
        put_o(patch + r32 * 36, o0, hi);
        put_o(patch + (r32 + 32) * 36, o1, hi);
        __builtin_amdgcn_fence(__ATOMIC_RELEASE, "wavefront");
        __builtin_amdgcn_wave_barrier();
        const u32x4 x0 = *(LDS_AS(u32x4)*)(patch + lane * 16), x1 = *(LDS_AS(u32x4)*)(patch + 1024 + lane * 16);
        const u32x4 x2 = *(LDS_AS(u32x4)*)(patch + 2048 + (lane & 15) * 16);
        store16(dst, lane * 16, x0);
        store16(dst, 1024 + lane * 16, x1);
        if (lane < 16) store16(dst, 2048 + lane * 16, x2);
    }
        __builtin_amdgcn_s_waitcnt(0x0F70);
    }
    STAMP_AT(5);
}

extern "C" void kernel_launch(void* const* d_in, const int* in_sizes, int n_in, void* d_out, int out_size, void* d_ws,
                              size_t ws_size, hipStream_t stream) {
    qk_norm_attn<<<256, 512, 0, stream>>>((const unsigned*)d_in[0], (const unsigned*)d_in[1], (const unsigned*)d_in[2],
                                          (const unsigned*)d_in[3], (unsigned*)d_out STAMP_PASS);
}
```

```cpp
#include <hip/hip_runtime.h>
#include <hip/hip_bf16.h>

typedef __attribute__((ext_vector_type(8))) __bf16 bf16x8;
typedef __attribute__((ext_vector_type(16))) __bf16 bf16x16;
typedef __attribute__((ext_vector_type(2))) __bf16 bf16x2;
typedef __attribute__((ext_vector_type(4))) short s16x4;
typedef __attribute__((ext_vector_type(2))) float f32x2;
typedef __attribute__((ext_vector_type(16))) float f32x16;
typedef __attribute__((ext_vector_type(32))) float f32x32;
typedef __attribute__((ext_vector_type(2))) unsigned u32x2;
typedef __attribute__((ext_vector_type(4))) unsigned u32x4;
typedef __attribute__((ext_vector_type(8))) unsigned u32x8;
typedef __attribute__((ext_vector_type(16))) unsigned u32x16;
typedef __attribute__((address_space(3))) char* lds_ptr;
typedef const __attribute__((address_space(1))) char* g_ptr;
#define G_AS(T) const __attribute__((address_space(1))) T
#define LDS_AS(T) __attribute__((address_space(3))) T
#define DI __device__ __forceinline__

constexpr int SEQ = 2048, NHEADS = 32;
constexpr int KX_OFF = 256;
constexpr int KM_OFF = KX_OFF + 8192;
constexpr int V_OFF = KM_OFF + 65536;
constexpr int LDS_BYTES = V_OFF + 256 * 320 + 1280;
constexpr float LOG2E = 1.4426950408889634f;
constexpr float SHIFT_SAFE = 40.0f;
constexpr unsigned ONE_ZERO = 0x00003f80u;

typedef u32x4 __attribute__((aligned(4))) u32x4_u;
struct Row9 { u32x4 a, b; unsigned c; };

DI Row9 load_row(g_ptr base, unsigned off) {
    Row9 r; g_ptr p = base + off;
    r.a = *(G_AS(u32x4_u)*)p; r.b = *(G_AS(u32x4_u)*)(p + 16); r.c = *(G_AS(unsigned)*)(p + 32);
    return r;
}
DI unsigned cvt_pk(float lo, float hi) { f32x2 f = {lo, hi}; return __builtin_bit_cast(unsigned, __builtin_convertvector(f, bf16x2)); }
DI float sq2(unsigned w, float acc) { return __builtin_amdgcn_fdot2_f32_bf16(__builtin_bit_cast(bf16x2, w), __builtin_bit_cast(bf16x2, w), acc, false); }
DI unsigned scale2(unsigned w, float g) { return cvt_pk(__uint_as_float(w << 16) * g, __uint_as_float(w & 0xffff0000u) * g); }
DI float inv_norm(float ss) { return __builtin_amdgcn_rcpf(__builtin_amdgcn_sqrtf(ss) + 1e-6f); }
DI g_ptr uniform_ptr(const void* p) {
    const unsigned long long a = (unsigned long long)p;
    const unsigned lo = __builtin_amdgcn_readfirstlane((unsigned)a), hi = __builtin_amdgcn_readfirstlane((unsigned)(a >> 32));
    return (g_ptr)(((unsigned long long)hi << 32) | lo);
}
DI void stage_k(lds_ptr pm, lds_ptr px, const Row9& r) {
    float s0 = sq2(r.a[0], 0.f), s1 = sq2(r.a[1], 0.f);
    s0 = sq2(r.a[2], s0); s1 = sq2(r.a[3], s1); s0 = sq2(r.b[0], s0); s1 = sq2(r.b[1], s1);
    s0 = sq2(r.b[2], s0); s1 = sq2(r.b[3], s1); s0 = sq2(r.c, s0);
    const float g = inv_norm(s0 + s1);
    *(LDS_AS(u32x4)*)pm = (u32x4){scale2(r.a[0], g), scale2(r.a[1], g), scale2(r.a[2], g), scale2(r.a[3], g)};
    *(LDS_AS(u32x4)*)(pm + 512) = (u32x4){scale2(r.b[0], g), scale2(r.b[1], g), scale2(r.b[2], g), scale2(r.b[3], g)};
    *(LDS_AS(unsigned)*)px = scale2(r.c, g);
}
DI void stage_v(lds_ptr pm, lds_ptr px, unsigned cval, const Row9& r) {
#define W2(w, s_lo, s_hi) *(LDS_AS(unsigned short)*)(pm + (s_lo) * 16) = (unsigned short)(w); *(LDS_AS(unsigned short)*)(pm + (s_hi) * 16) = (unsigned short)((w) >> 16)
    W2(r.a[0], 0, 1); W2(r.a[1], 2, 3); W2(r.a[2], 4, 5); W2(r.a[3], 6, 7);
    W2(r.b[0], 8, 9); W2(r.b[1], 10, 19); W2(r.b[2], 12, 13); W2(r.b[3], 14, 15);
    W2(r.c, 16, 17);
#undef W2
    *(LDS_AS(unsigned)*)px = cval;
}
DI float prep_q(const u32x4 a, const unsigned e, float c, u32x4& qm) {
    float s0 = sq2(a[0], 0.f), s1 = sq2(a[1], 0.f);
    s0 = sq2(a[2], s0); s1 = sq2(a[3], s1); s0 = sq2(e, s0);
    s0 += s1;
    auto sw = __builtin_amdgcn_permlane32_swap(__float_as_uint(s0), __float_as_uint(s0), false, false);
    const float g = c * inv_norm(__uint_as_float(sw[0]) + __uint_as_float(sw[1]));
    qm = (u32x4){scale2(a[0], g), scale2(a[1], g), scale2(a[2], g), scale2(a[3], g)};
    return g;
}
DI void put_o(lds_ptr p, const f32x16& o, int hi) {
    auto sw = __builtin_amdgcn_permlane32_swap(__float_as_uint(o[10]), __float_as_uint(o[10]), false, false);
    const float rl = __builtin_amdgcn_rcpf(__uint_as_float(sw[0]));
    LDS_AS(unsigned)* w = (LDS_AS(unsigned)*)(p + 8 * hi);
    w[0] = cvt_pk(o[0] * rl, o[1] * rl); w[1] = cvt_pk(o[2] * rl, o[3] * rl);
    w[4] = cvt_pk(o[4] * rl, o[5] * rl); w[5] = cvt_pk(o[6] * rl, o[7] * rl);
    if (!hi) w[8] = cvt_pk(o[8] * rl, o[9] * rl);
}
DI void store16(g_ptr base, unsigned off, u32x4 x) {
    asm volatile("global_store_dwordx4 %0, %1, %2 sc1\n\ts_nop 1" :: "v"(off), "v"(x), "s"(base) : "memory");
}
#define LO16(x) __builtin_shufflevector(x, x, 0, 1, 2, 3, 4, 5, 6, 7, 8, 9, 10, 11, 12, 13, 14, 15)
#define HI16(x) __builtin_shufflevector(x, x, 16, 17, 18, 19, 20, 21, 22, 23, 24, 25, 26, 27, 28, 29, 30, 31)
#define LO8(x) __builtin_bit_cast(bf16x16, __builtin_shufflevector(x, x, 0, 1, 2, 3, 4, 5, 6, 7))
#define HI8(x) __builtin_bit_cast(bf16x16, __builtin_shufflevector(x, x, 8, 9, 10, 11, 12, 13, 14, 15))
#define SMF(a, b, c) __builtin_amdgcn_smfmac_f32_32x32x32_bf16(a, b, c, sel, 0, 0)
#define MF16(a, b, c) __builtin_amdgcn_mfma_f32_32x32x16_bf16(a, b, c, 0, 0, 0)
#define PIN(x) asm volatile("" : "+v"(x))
#define LDB128(p) (*(const LDS_AS(bf16x8)*)(p))

#define EX(C, i) "v_exp_f32 v[" #C "+" #i "], v[" #C "+" #i "]\n\t"
#define EXL(C, i) "v_exp_f32_e64 v[" #C "+" #i "], v[" #C "+" #i "]\n\t"
#define EX2(C, a, b) EX(C, a) EX(C, b)
#define EX3(C, a, b, c) EX2(C, a, b) EXL(C, c)
#define EX5(C, a, b, c, d, e) EX2(C, a, b) EX3(C, c, d, e)
#define EX8(C, a, b, c, d, e, f, g, h) EX2(C, a, b) EX2(C, c, d) EX2(C, e, f) EX2(C, g, h)
#define PV(O, V, P, i) "v_smfmac_f32_32x32x32_bf16 v[" #O ":" #O "+15], v[" #V ":" #V "+3], v[" #P "+" #i ":" #P "+" #i "+7], v132\n\t"
#define QK(N, i, K, Q) "v_mfma_f32_32x32x16_bf16 v[" #N "+" #i ":" #N "+" #i "+15], v[" #K ":" #K "+3], v[" #Q ":" #Q "+3], v[" #N "+" #i ":" #N "+" #i "+15]\n\t"
#define QX(N, ab) "v_mfma_f32_32x32x4_2b_bf16 v[" #N ":" #N "+31], v[128:129], v[130:131], 0 cbsz:1 abid:" #ab "\n\t"
#define LD128(D, A, off) "ds_read_b128 v[" #D ":" #D "+3], " A " offset:" off "\n\t"
#define NOP "s_nop 0\n\t"
#define HD(p, w, C) "s_setprio " #p "\n\ts_waitcnt " w "lgkmcnt(0)\n\t" EX2(C, 0, 1)
#define STEP(j, P, W, C, N, VC0, VC1, VN0, VN1, KU, KL, ab, HA, HB, H1, H2, H3, H4) \
    HD(P, W, C) PV(16, VN1, N, 24) LD128(VN0, "v135", "(" #j "+1)*1280") EX3(C, 2, 3, 4) \
    QX(N, ab) LD128(VN1, "v135", "(" #j "+1)*1280+640") HA EX8(C, 5, 6, 7, 8, 9, 10, 11, 12) \
    HB PV(0, VC0, C, 0) LD128(KL, "v133", "(" #j "+2)*1024") EX5(C, 13, 14, 15, 16, 17) \
    PV(0, VC1, C, 8) H1 EX3(C, 18, 19, 20) \
    QK(N, 0, KU, 96) H2 EX5(C, 21, 22, 23, 24, 25) PV(16, VC0, C, 16) H3 EX3(C, 26, 27, 28) QK(N, 16, KU, 100) H4 EX3(C, 29, 30, 31)
#define KXL(j) "ds_read_b32 v128, v134 offset:(" #j "+2)*128\n\t"
#define STEP_EVEN(j, P, W, H2, H3, H4) STEP(j, P, W, 32, 64, 104, 108, 112, 116, 124, 120, 1, "", "", KXL(j), H2, H3, H4)
#define STEP_ODD(j, P, W, H1, H2, H3, H4) STEP(j, P, W, 64, 32, 112, 116, 104, 108, 120, 124, 0, "", "", H1, H2, H3, H4)
#define DOT(d, a, c) "v_dot2_f32_bf16 v" #d ", v" #a ", v" #a ", " c "\n\t"
#define UNP(d, r) "v_mfma_f32_4x4x4_16b_bf16 v[" #d ":" #d "+3], v[172:173], v[" #r ":" #r "+1], 0\n\t"
#define K_NORM DOT(150, 136, "0") DOT(151, 137, "0") DOT(150, 138, "v150") DOT(151, 139, "v151") DOT(150, 140, "v150") \
    DOT(151, 141, "v151") DOT(150, 142, "v150") DOT(151, 143, "v151") DOT(150, 144, "v150") \
    UNP(152, 136) UNP(156, 138) UNP(160, 140) "v_add_f32 v150, v150, v151\n\tv_sqrt_f32 v150, v150\n\t" UNP(164, 142) \
    "v_add_f32 v150, 0x358637bd, v150\n\tv_rcp_f32_e64 v150, v150\n\t" UNP(168, 144)
#define PKM(x) "v_pk_mul_f32 v[" #x ":" #x "+1], v[150:151], v[" #x ":" #x "+1] op_sel_hi:[0,1]\n\t"
#define CVT(d, x) "v_cvt_pk_bf16_f32 v[" #d "], v[" #x "], v[" #x "+1]\n\t"
#define K_PK8(x) PKM(x) PKM(x + 2) PKM(x + 4) PKM(x + 6) CVT(x, x) CVT(x + 1, x + 2) CVT(x + 2, x + 4) CVT(x + 3, x + 6)
#define SITE(n, PART) "s_cbranch_scc1 .Lqk_s" #n "_%=\n\t" NOP PART ".Lqk_s" #n "_%=:\n\t"
#define WL(r, s) "ds_write_b16 v146, v" #r " offset:" #s "*16\n\t"
#define WH(r, s) "ds_write_b16_d16_hi v146, v" #r " offset:" #s "*16\n\t"
#define LDA "global_load_dwordx4 v[136:139], v149, s[20:21]\n\t"
#define LDB "global_load_dwordx4 v[140:143], v149, s[20:21] offset:16\n\t"
#define LDC "global_load_dword v144, v149, s[20:21] offset:32\n\t"
#define FLAG "ds_write_b32 v181, v179\n\t"
#define BODY_TOP(r) \
    ".p2align 6\n" \
    ".Lqk_top_" #r "_%=:\n\t" \
    STEP(0, 3, "", 32, 64, 104, 108, 112, 116, 124, 120, 1, "v_cmp_lt_u32_e64 vcc, v180, v179\n\t", \
         "s_cbranch_vccnz .Lqk_spin_" #r "_%=\n\t" NOP ".Lqk_go_" #r "_%=:\n\t", KXL(0), "", "", "") \
    "s_cmp_eq_u32 %[it], 8\n\ts_cbranch_scc1 .Lqk_end_%=\n\t" \
    STEP_ODD(1, 3, "", "", "v_add_u32_e64 v179, 1, v179\n\t", "", "") \
    ".Lqk_mid_" #r "_%=:\n\t" \
    "s_cmp_eq_u32 %[it], 7\n\t" NOP
#define BODY_END(r) \
    STEP_ODD(7, 0, "", "s_cmp_eq_u32 %[it], 6\n\ts_cselect_b32 %[tmp], -7, 1\n\t", "ds_read_b32 v180, v182\n\tv_mad_i32_i24 v146, %[tmp], v177, v146\n\t", \
             "v_mad_i32_i24 v147, %[tmp], v178, v147\n\t", "v_add_u32 v133, 0x2000, v133\n\t") \
    "v_add_u32 v134, 0x400, v134\n\tv_add_u32 v135, 0x2800, v135\n\t" \
    "s_add_u32 %[it], %[it], 1\n\ts_cmp_lt_u32 %[it], 6\n\ts_cselect_b32 %[tmp], 0x2400, 0\n\t"        \
    "s_add_u32 s20, s20, %[tmp]\n\ts_addc_u32 s21, s21, 0\n\t" \
    "s_branch .Lqk_top_" #r "_%=\n" \
    ".Lqk_spin_" #r "_%=:\n\ts_setprio 0\n\tds_read_b32 v180, v182\n\ts_waitcnt lgkmcnt(0)\n\tv_cmp_lt_u32 vcc, v180, v179\n\ts_nop 4\n\t" \
    "s_cbranch_vccnz .Lqk_spin_" #r "_%=\n\ts_setprio 3\n\ts_branch .Lqk_go_" #r "_%=\n\t"
#define BODY_K BODY_TOP(k) \
    STEP_EVEN(2, 3, "vmcnt(0) ", SITE(a, K_NORM), "", LDA) \
    STEP_ODD(3, 2, "", LDB, SITE(b, K_PK8(152)), "ds_write_b128 v146, v[152:155]\n\t", LDC) \
    STEP_EVEN(4, 2, "", SITE(c, K_PK8(160)), "ds_write_b128 v146, v[160:163] offset:512\n\t", "") \
    STEP_ODD(5, 1, "", "", SITE(d, PKM(168) CVT(168, 168)), "ds_write_b32 v147, v168\n\t", FLAG) \
    STEP_EVEN(6, 1, "", "", "", "") BODY_END(k)
#define BODY_V BODY_TOP(v) \
    STEP_EVEN(2, 3, "vmcnt(2) ", WL(136, 0), WH(136, 1) WL(137, 2), WH(137, 3)) \
    STEP_ODD(3, 2, "", WL(138, 4), WH(138, 5), WL(139, 6) WH(139, 7), LDA) \
    STEP_EVEN(4, 2, "vmcnt(2) ", WL(140, 8), WH(140, 9) WL(141, 10), WH(141, 19)) \
    STEP_ODD(5, 1, "", WL(142, 12), WH(142, 13), WL(143, 14) WH(143, 15), LDB) \
    STEP_EVEN(6, 1, "vmcnt(2) ", WL(144, 16), WH(144, 17) "ds_write_b32 v147, v148\n\t", FLAG LDC) BODY_END(v)
#define OMUL(O, i) "v_pk_mul_f32 v[" #O "+" #i ":" #O "+" #i "+1], v[" #O "+10:" #O "+11], v[" #O "+" #i ":" #O "+" #i "+1] op_sel_hi:[0,1]\n\t"
#define OCVT(O, d, i) "v_cvt_pk_bf16_f32 v[" #O "+" #d "], v[" #O "+" #i "], v[" #O "+" #i "+1]\n\t"
#define OUT_HEAD(O) \
    "v_mov_b32 v[" #O "+11], v[" #O "+10]\n\ts_nop 1\n\tv_permlane32_swap_b32 v[" #O "+10], v[" #O "+11]\n\tv_rcp_f32 v[" #O "+10], v[" #O "+10]\n\ts_nop 0\n\t"
#define OUT_REST(O, A) \
    OMUL(O, 0) OMUL(O, 2) OMUL(O, 4) OMUL(O, 6) OMUL(O, 8) OCVT(O, 0, 0) OCVT(O, 1, 2) OCVT(O, 2, 4) OCVT(O, 3, 6) OCVT(O, 4, 8) \
    "ds_write2_b32 v" #A ", v[" #O "], v[" #O "+1] offset1:1\n\tds_write2_b32 v" #A ", v[" #O "+2], v[" #O "+3] offset0:4 offset1:5\n\t" \
    "s_mov_b32 exec_hi, 0\n\tds_write_b32 v" #A ", v[" #O "+4] offset:32\n\ts_mov_b32 exec_hi, -1\n\t"
#define LDROW(a, b, c, off, base) \
    "global_load_dwordx4 " a ", " off ", " base "\n\tglobal_load_dwordx4 " b ", " off ", " base " offset:16\n\tglobal_load_dword " c ", " off ", " base " offset:32\n\t"

__global__ __launch_bounds__(512) void qk_norm_attn(const unsigned* __restrict__ q, const unsigned* __restrict__ k,
                                                    const unsigned* __restrict__ v, const unsigned* __restrict__ t,
                                                    unsigned* __restrict__ out  ) {
    __shared__ __attribute__((aligned(16))) char smem[LDS_BYTES];
    lds_ptr lds = (lds_ptr)smem;
    const int tid = threadIdx.x, lane = tid & 63, r32 = lane & 31, hi = lane >> 5;
    const int wid = __builtin_amdgcn_readfirstlane(tid >> 6);
    const int b = blockIdx.x;
    const int head = (b & 7) + 8 * (b >> 5), qc = (b >> 3) & 3;
    const size_t hbase = (size_t)head * SEQ * 9;
    const int qrow = qc * 512 + wid * 64 + r32;
    const bool is_k = wid < 4;
    const int srow = tid & 255;
    g_ptr src = uniform_ptr((is_k ? k : v) + hbase);
    g_ptr qsrc = uniform_ptr(q + hbase);
    g_ptr dst = uniform_ptr(out + hbase + (size_t)(qc * 512 + wid * 64) * 9);
    const unsigned tw = t[(head & (NHEADS - 1)) >> 1];
    unsigned soff = srow * 36;
    const unsigned qoffh = qrow * 36 + 16 * hi, qoffx = qrow * 36 + 1152 * hi;
    Row9 raw, nxt; u32x4 qa0, qa1; unsigned qb;
    asm volatile("global_load_dword %4, %[qx], %[qs] offset:32\n\tglobal_load_dwordx4 %3, %[qh], %[qs]\n\t"
                 "global_load_dwordx4 %5, %[qh], %[qs] offset:1152\n\t" LDROW("%0", "%1", "%2", "%[so]", "%[src]")
                 : "=&v"(raw.a), "=&v"(raw.b), "=&v"(raw.c), "=&v"(qa0), "=&v"(qb), "=&v"(qa1)
                 : [so] "v"(soff), [src] "s"(src), [qh] "v"(qoffh), [qx] "v"(qoffx), [qs] "s"(qsrc) : "memory");
    int sm_step = is_k ? 8192 : 10240, sx_step = is_k ? 1024 : 10240;
    const int vrow = V_OFF + (srow >> 3) * 320;
    lds_ptr sm = lds + (is_k ? KM_OFF + (srow >> 5) * 1024 + (srow & 31) * 16 : vrow + (srow & 7) * 2);
    lds_ptr sx = lds + (is_k ? KX_OFF + srow * 4 : vrow + ((srow & 4) ? 11 * 16 : 18 * 16) + (srow & 3) * 4);
    unsigned cval = (srow & 4) ? 0u : 0x3f803f80u;
    const float c = __uint_as_float((head & 1) ? (tw & 0xffff0000u) : (tw << 16)) * LOG2E;
    const bool fast = __builtin_fabsf(c) <= SHIFT_SAFE;
    const unsigned shift_bits = fast ? (cvt_pk(-__builtin_fabsf(c), 0.f) & 0xffffu) : 0u;
    const int vslot = r32 == 11 ? 19 : (r32 > 18 ? 11 : r32);
    lds_ptr kmp = lds + KM_OFF - 2048 + lane * 16;
    lds_ptr kxp = lds + KX_OFF - 256 + lane * 4;
    lds_ptr vp = lds + V_OFF - 2560 + hi * 320 + vslot * 16;
    int sel = 0xDDDD;
    lds_ptr patch = lds + V_OFF + 20480 + wid * 2304;
    lds_ptr orow = patch + r32 * 36 + 8 * hi, olin = patch + lane * 16, fown = lds + wid * 4, fall = lds + (lane & 7) * 4;
    u32x2 ident = (u32x2){(lane & 2) ? 0u : (0x3f80u << (16 * (lane & 1))), (lane & 2) ? (0x3f80u << (16 * (lane & 1))) : 0u};
    u32x2 kx = (u32x2){0u, ONE_ZERO};
    int token = 1, zero = 0, ooff = lane * 16;
    f32x16 o0, o1; u32x8 pz; u32x4 vz;
#pragma unroll
    for (int i = 0; i < 16; ++i) { o0[i] = 0.f; o1[i] = 0.f; }
#pragma unroll
    for (int i = 0; i < 8; ++i) { pz[i] = 0u; vz[i & 3] = 0u; }
    asm volatile("" : "+{v[0:15]}"(o0), "+{v[16:31]}"(o1), "+{v[88:95]}"(pz), "+{v[116:119]}"(vz), "+{v[128:129]}"(kx), "+{v132}"(sel),
                 "+{v133}"(kmp), "+{v134}"(kxp), "+{v135}"(vp), "+{v145}"(zero), "+{v148}"(cval), "+{v149}"(soff), "+{v[172:173]}"(ident),
                 "+{v174}"(orow), "+{v175}"(olin), "+{v176}"(ooff), "+{v177}"(sm_step), "+{v178}"(sx_step), "+{v179}"(token),
                 "+{v181}"(fown), "+{v182}"(fall), "+s"(dst));
    u32x4 qm0, qm1;
    asm volatile("s_waitcnt vmcnt(4)" : "+v"(qa0), "+v"(qb));
    const float g0 = prep_q(qa0, hi ? 0u : qb, c, qm0);
    asm volatile("s_waitcnt vmcnt(3)" : "+v"(qa1));
    const float g1 = prep_q(qa1, hi ? qb : 0u, c, qm1);
    u32x2 qx = (u32x2){scale2(qb, hi ? g1 : g0), shift_bits};
    PIN(qm0); PIN(qm1); PIN(qx);
    asm volatile("s_waitcnt vmcnt(0)\n\t" LDROW("v[136:139]", "v[140:143]", "v144", "v149", "%[src]")
                 : "=&{v[136:139]}"(nxt.a), "=&{v[140:143]}"(nxt.b), "=&{v144}"(nxt.c), "+v"(raw.a), "+v"(raw.b), "+v"(raw.c)
                 : "{v149}"(soff), [src] "s"(src + 9216) : "memory");
    if (is_k) stage_k(sm, sx, raw); else stage_v(sm, sx, cval, raw);
    sm += sm_step; sx += sx_step;
    *(LDS_AS(int)*)fown = 0;
    __syncthreads();

    if (__builtin_expect(fast, 1)) {
        int tmp, it = 0;
        f32x32 sa; u32x16 sb0; u32x8 sb1, vf0, kf; u32x4 vf1;
        unsigned long long nsrc = (unsigned long long)(src + 2 * 9216);
        asm volatile(
            "ds_read_b32 v128, v134 offset:256\n\t" LD128(120, "v133", "2048") LD128(124, "v133", "3072")
            LD128(104, "v135", "2560") LD128(108, "v135", "3200")
            "s_waitcnt lgkmcnt(4)\n\t" QX(32, 0) "s_nop 15\n\ts_nop 1\n\ts_waitcnt lgkmcnt(0)\n\t" QK(32, 0, 120, 96) QK(32, 16, 120, 100) "s_nop 7\n\t"
            "s_cmp_gt_u32 %[wid], 3\n\ts_cbranch_scc1 .Lqk_mid_v_%=\n\ts_branch .Lqk_mid_k_%=\n\t"
            BODY_K BODY_V
            ".p2align 5\n"
            ".Lqk_end_%=:\n\t"
            HD(0, "", 64) PV(16, 108, 32, 24) EX8(64, 2, 3, 4, 5, 6, 7, 8, 9) PV(0, 112, 64, 0) EX8(64, 10, 11, 12, 13, 14, 15, 16, 17)
            PV(0, 116, 64, 8) EX8(64, 18, 19, 20, 21, 22, 23, 24, 25) PV(16, 112, 64, 16) EX2(64, 26, 27) EX2(64, 28, 29) EX2(64, 30, 31)
            OUT_HEAD(0) PV(16, 116, 64, 24) OUT_REST(0, 174)
            "v_add_u32 v174, 1152, v174\n\t"
            OUT_HEAD(16) OUT_REST(16, 174)
            "s_waitcnt lgkmcnt(0)\n\t"
            "ds_read_b128 v[32:35], v175\n\tds_read_b128 v[36:39], v175 offset:1024\n\tds_read_b128 v[40:43], v175 offset:2048\n\t"
            "s_waitcnt lgkmcnt(2)\n\tglobal_store_dwordx4 v176, v[32:35], %[dst] sc1\n\t"
            "s_waitcnt lgkmcnt(1)\n\tglobal_store_dwordx4 v176, v[36:39], %[dst] offset:1024 sc1\n\t"
            "s_mov_b64 exec, 0xffff\n\t"
            "s_waitcnt lgkmcnt(0)\n\tglobal_store_dwordx4 v176, v[40:43], %[dst] offset:2048 sc1\n\t"
            "s_mov_b64 exec, -1\n\ts_nop 1"
            : "+{v[0:15]}"(o0), "+{v[16:31]}"(o1), "=&{v[32:63]}"(sa), "=&{v[64:79]}"(sb0), "=&{v[80:87]}"(sb1), "+{v[88:95]}"(pz),
              "=&{v[104:111]}"(vf0), "=&{v[112:115]}"(vf1), "+{v[116:119]}"(vz), "=&{v[120:127]}"(kf),
              "+{v[128:129]}"(kx), "+{v133}"(kmp), "+{v134}"(kxp), "+{v135}"(vp), "+{v[136:139]}"(nxt.a), "+{v[140:143]}"(nxt.b),
              "+{v144}"(nxt.c), "+{v146}"(sm), "+{v147}"(sx), "+{v174}"(orow), "+{v179}"(token), [it] "+s"(it), "+{s[20:21]}"(nsrc),
              [tmp] "=&s"(tmp)
            : "{v[96:99]}"(qm0), "{v[100:103]}"(qm1), "{v[130:131]}"(qx), "{v132}"(sel), "{v145}"(zero), "{v148}"(cval), "{v149}"(soff),
              "{v[172:173]}"(ident), "{v175}"(olin), "{v176}"(ooff), "{v177}"(sm_step), "{v178}"(sx_step),
              "{v181}"(fown), "{v182}"(fall), [dst] "s"(dst), [wid] "s"(wid)
            : "memory", "scc", "vcc", "v150", "v151", "v152", "v153", "v154", "v155", "v156", "v157", "v158", "v159", "v160", "v161",
              "v162", "v163", "v164", "v165", "v166", "v167", "v168", "v169", "v170", "v171", "v180");
    } else {
        asm volatile("s_waitcnt vmcnt(0)" :: "{v[136:139]}"(nxt.a), "{v[140:143]}"(nxt.b), "{v144}"(nxt.c) : "memory");
        float m0 = -INFINITY, m1 = -INFINITY;
        kmp += 2048; vp += 2560;
        kxp = lds + KX_OFF + r32 * 4;
        const bf16x8 q0 = __builtin_bit_cast(bf16x8, qm0), q1 = __builtin_bit_cast(bf16x8, qm1);
        const s16x4 qxs = __builtin_bit_cast(s16x4, qx);
#pragma unroll 1
        for (int i = 0; i < 64; ++i) {
            if ((i & 7) == 0 && i > 0) {
                raw = load_row(src + (i >> 3) * 9216, soff);
                if (is_k) stage_k(sm, sx, raw); else stage_v(sm, sx, cval, raw);
                sm += sm_step; sx += sx_step;
                __syncthreads();
            }
            const bf16x8 kf = LDB128(kmp), v0 = LDB128(vp), v1 = LDB128(vp + 640);
            const s16x4 kx = __builtin_bit_cast(s16x4, (u32x2){*(const LDS_AS(unsigned)*)kxp, ONE_ZERO});
            f32x32 zero32;
#pragma unroll
            for (int j = 0; j < 32; ++j) zero32[j] = 0.f;
            const f32x32 sx32 = __builtin_amdgcn_mfma_f32_32x32x4bf16_1k(kx, qxs, zero32, 0, 0, 0);
            f32x16 s0 = MF16(kf, q0, LO16(sx32)), s1 = MF16(kf, q1, HI16(sx32));
#define ONLINE(S, M, O) do { float mx = S[0]; _Pragma("unroll") for (int j = 1; j < 16; ++j) mx = __builtin_fmaxf(mx, S[j]); \
                auto sw = __builtin_amdgcn_permlane32_swap(__float_as_uint(mx), __float_as_uint(mx), false, false); \
                const float mn = __builtin_fmaxf(M, __builtin_fmaxf(__uint_as_float(sw[0]), __uint_as_float(sw[1]))); \
                const float alpha = __builtin_amdgcn_exp2f(M - mn); M = mn; \
                _Pragma("unroll") for (int j = 0; j < 16; ++j) { O[j] *= alpha; S[j] = __builtin_amdgcn_exp2f(S[j] - mn); } \
                O = SMF(v0, LO8(S), O); O = SMF(v1, HI8(S), O); } while (0)
            ONLINE(s0, m0, o0);
            ONLINE(s1, m1, o1);
            kmp += 1024; kxp += 128; vp += 1280;
        }
        put_o(patch + r32 * 36, o0, hi);
        put_o(patch + (r32 + 32) * 36, o1, hi);
        __builtin_amdgcn_fence(__ATOMIC_RELEASE, "wavefront");
        __builtin_amdgcn_wave_barrier();
        const u32x4 x0 = *(LDS_AS(u32x4)*)(patch + lane * 16), x1 = *(LDS_AS(u32x4)*)(patch + 1024 + lane * 16);
        const u32x4 x2 = *(LDS_AS(u32x4)*)(patch + 2048 + (lane & 15) * 16);
        store16(dst, lane * 16, x0);
        store16(dst, 1024 + lane * 16, x1);
        if (lane < 16) store16(dst, 2048 + lane * 16, x2);
        __builtin_amdgcn_s_waitcnt(0x0F70);
    }
}

extern "C" void kernel_launch(void* const* d_in, const int* in_sizes, int n_in, void* d_out, int out_size, void* d_ws,
                              size_t ws_size, hipStream_t stream) {
    qk_norm_attn<<<256, 512, 0, stream>>>((const unsigned*)d_in[0], (const unsigned*)d_in[1], (const unsigned*)d_in[2],
                                          (const unsigned*)d_in[3], (unsigned*)d_out  );
}
```

```cpp
#include <hip/hip_runtime.h>
#include <hip/hip_bf16.h>

typedef __attribute__((ext_vector_type(8))) __bf16 bf16x8;
typedef __attribute__((ext_vector_type(16))) __bf16 bf16x16;
typedef __attribute__((ext_vector_type(2))) __bf16 bf16x2;
typedef __attribute__((ext_vector_type(4))) short s16x4;
typedef __attribute__((ext_vector_type(2))) float f32x2;
typedef __attribute__((ext_vector_type(16))) float f32x16;
typedef __attribute__((ext_vector_type(32))) float f32x32;
typedef __attribute__((ext_vector_type(2))) unsigned u32x2;
typedef __attribute__((ext_vector_type(4))) unsigned u32x4;
typedef __attribute__((ext_vector_type(8))) unsigned u32x8;
typedef __attribute__((ext_vector_type(16))) unsigned u32x16;
typedef __attribute__((address_space(3))) char* lds_ptr;
typedef const __attribute__((address_space(1))) char* g_ptr;
#define G_AS(T) const __attribute__((address_space(1))) T
#define LDS_AS(T) __attribute__((address_space(3))) T
#define DI __device__ __forceinline__

constexpr int SEQ = 2048, NHEADS = 32;
constexpr int KX_OFF = 256;
constexpr int KM_OFF = KX_OFF + 8192;
constexpr int V_OFF = KM_OFF + 65536;
constexpr int LDS_BYTES = V_OFF + 256 * 320 + 1280;
constexpr float LOG2E = 1.4426950408889634f;
constexpr float SHIFT_SAFE = 40.0f;
constexpr unsigned ONE_ZERO = 0x00003f80u;

typedef u32x4 __attribute__((aligned(4))) u32x4_u;
struct Row9 { u32x4 a, b; unsigned c; };

DI Row9 load_row(g_ptr base, unsigned off) {
    Row9 r; g_ptr p = base + off;
    r.a = *(G_AS(u32x4_u)*)p; r.b = *(G_AS(u32x4_u)*)(p + 16); r.c = *(G_AS(unsigned)*)(p + 32);
    return r;
}
DI unsigned cvt_pk(float lo, float hi) { f32x2 f = {lo, hi}; return __builtin_bit_cast(unsigned, __builtin_convertvector(f, bf16x2)); }
DI float sq2(unsigned w, float acc) { return __builtin_amdgcn_fdot2_f32_bf16(__builtin_bit_cast(bf16x2, w), __builtin_bit_cast(bf16x2, w), acc, false); }
DI unsigned scale2(unsigned w, float g) { return cvt_pk(__uint_as_float(w << 16) * g, __uint_as_float(w & 0xffff0000u) * g); }
DI float inv_norm(float ss) { return __builtin_amdgcn_rcpf(__builtin_amdgcn_sqrtf(ss) + 1e-6f); }
DI g_ptr uniform_ptr(const void* p) {
    const unsigned long long a = (unsigned long long)p;
    const unsigned lo = __builtin_amdgcn_readfirstlane((unsigned)a), hi = __builtin_amdgcn_readfirstlane((unsigned)(a >> 32));
    return (g_ptr)(((unsigned long long)hi << 32) | lo);
}
DI void stage_k(lds_ptr pm, lds_ptr px, const Row9& r) {
    float s0 = sq2(r.a[0], 0.f), s1 = sq2(r.a[1], 0.f);
    s0 = sq2(r.a[2], s0); s1 = sq2(r.a[3], s1); s0 = sq2(r.b[0], s0); s1 = sq2(r.b[1], s1);
    s0 = sq2(r.b[2], s0); s1 = sq2(r.b[3], s1); s0 = sq2(r.c, s0);
    const float g = inv_norm(s0 + s1);
    *(LDS_AS(u32x4)*)pm = (u32x4){scale2(r.a[0], g), scale2(r.a[1], g), scale2(r.a[2], g), scale2(r.a[3], g)};
    *(LDS_AS(u32x4)*)(pm + 512) = (u32x4){scale2(r.b[0], g), scale2(r.b[1], g), scale2(r.b[2], g), scale2(r.b[3], g)};
    *(LDS_AS(unsigned)*)px = scale2(r.c, g);
}
DI void stage_v(lds_ptr pm, lds_ptr px, unsigned cval, const Row9& r) {
#define W2(w, s_lo, s_hi) *(LDS_AS(unsigned short)*)(pm + (s_lo) * 16) = (unsigned short)(w); *(LDS_AS(unsigned short)*)(pm + (s_hi) * 16) = (unsigned short)((w) >> 16)
    W2(r.a[0], 0, 1); W2(r.a[1], 2, 3); W2(r.a[2], 4, 5); W2(r.a[3], 6, 7);
    W2(r.b[0], 8, 9); W2(r.b[1], 10, 19); W2(r.b[2], 12, 13); W2(r.b[3], 14, 15);
    W2(r.c, 16, 17);
#undef W2
    *(LDS_AS(unsigned)*)px = cval;
}
DI float prep_q(const u32x4 a, const unsigned e, float c, u32x4& qm) {
    float s0 = sq2(a[0], 0.f), s1 = sq2(a[1], 0.f);
    s0 = sq2(a[2], s0); s1 = sq2(a[3], s1); s0 = sq2(e, s0);
    s0 += s1;
    auto sw = __builtin_amdgcn_permlane32_swap(__float_as_uint(s0), __float_as_uint(s0), false, false);
    const float g = c * inv_norm(__uint_as_float(sw[0]) + __uint_as_float(sw[1]));
    qm = (u32x4){scale2(a[0], g), scale2(a[1], g), scale2(a[2], g), scale2(a[3], g)};
    return g;
}
DI void put_o(lds_ptr p, const f32x16& o, int hi) {
    auto sw = __builtin_amdgcn_permlane32_swap(__float_as_uint(o[10]), __float_as_uint(o[10]), false, false);
    const float rl = __builtin_amdgcn_rcpf(__uint_as_float(sw[0]));
    LDS_AS(unsigned)* w = (LDS_AS(unsigned)*)(p + 8 * hi);
    w[0] = cvt_pk(o[0] * rl, o[1] * rl); w[1] = cvt_pk(o[2] * rl, o[3] * rl);
    w[4] = cvt_pk(o[4] * rl, o[5] * rl); w[5] = cvt_pk(o[6] * rl, o[7] * rl);
    if (!hi) w[8] = cvt_pk(o[8] * rl, o[9] * rl);
}
DI void store16(g_ptr base, unsigned off, u32x4 x) {
    asm volatile("global_store_dwordx4 %0, %1, %2 sc1\n\ts_nop 1" :: "v"(off), "v"(x), "s"(base) : "memory");
}
#define LO16(x) __builtin_shufflevector(x, x, 0, 1, 2, 3, 4, 5, 6, 7, 8, 9, 10, 11, 12, 13, 14, 15)
#define HI16(x) __builtin_shufflevector(x, x, 16, 17, 18, 19, 20, 21, 22, 23, 24, 25, 26, 27, 28, 29, 30, 31)
#define LO8(x) __builtin_bit_cast(bf16x16, __builtin_shufflevector(x, x, 0, 1, 2, 3, 4, 5, 6, 7))
#define HI8(x) __builtin_bit_cast(bf16x16, __builtin_shufflevector(x, x, 8, 9, 10, 11, 12, 13, 14, 15))
#define SMF(a, b, c) __builtin_amdgcn_smfmac_f32_32x32x32_bf16(a, b, c, sel, 0, 0)
#define MF16(a, b, c) __builtin_amdgcn_mfma_f32_32x32x16_bf16(a, b, c, 0, 0, 0)
#define PIN(x) asm volatile("" : "+v"(x))
#define LDB128(p) (*(const LDS_AS(bf16x8)*)(p))

#define EX(C, i) "v_exp_f32 v[" #C "+" #i "], v[" #C "+" #i "]\n\t"
#define EXL(C, i) "v_exp_f32_e64 v[" #C "+" #i "], v[" #C "+" #i "]\n\t"
#define EX2(C, a, b) EX(C, a) EX(C, b)
#define EX3(C, a, b, c) EX2(C, a, b) EXL(C, c)
#define EX5(C, a, b, c, d, e) EX2(C, a, b) EX3(C, c, d, e)
#define EX8(C, a, b, c, d, e, f, g, h) EX2(C, a, b) EX2(C, c, d) EX2(C, e, f) EX2(C, g, h)
#define PV(O, V, P, i) "v_smfmac_f32_32x32x32_bf16 v[" #O ":" #O "+15], v[" #V ":" #V "+3], v[" #P "+" #i ":" #P "+" #i "+7], v132\n\t"
#define QK(N, i, K, Q) "v_mfma_f32_32x32x16_bf16 v[" #N "+" #i ":" #N "+" #i "+15], v[" #K ":" #K "+3], v[" #Q ":" #Q "+3], v[" #N "+" #i ":" #N "+" #i "+15]\n\t"
#define QX(N, ab) "v_mfma_f32_32x32x4_2b_bf16 v[" #N ":" #N "+31], v[128:129], v[130:131], 0 cbsz:1 abid:" #ab "\n\t"
#define LD128(D, A, off) "ds_read_b128 v[" #D ":" #D "+3], " A " offset:" off "\n\t"
#define NOP "s_nop 0\n\t"
#define HD(p, w, C) "s_setprio " #p "\n\ts_waitcnt " w "lgkmcnt(0)\n\t" EX2(C, 0, 1)
#define STEP(j, P, W, C, N, VC0, VC1, VN0, VN1, KU, KL, ab, HA, HB, H1, H2, H3, H4) \
    HD(P, W, C) PV(16, VN1, N, 24) LD128(VN0, "v135", "(" #j "+1)*1280") EX3(C, 2, 3, 4) \
    QX(N, ab) LD128(VN1, "v135", "(" #j "+1)*1280+640") HA EX8(C, 5, 6, 7, 8, 9, 10, 11, 12) \
    HB PV(0, VC0, C, 0) LD128(KL, "v133", "(" #j "+2)*1024") EX5(C, 13, 14, 15, 16, 17) \
    PV(0, VC1, C, 8) H1 EX3(C, 18, 19, 20) \
    QK(N, 0, KU, 96) H2 EX5(C, 21, 22, 23, 24, 25) PV(16, VC0, C, 16) H3 EX3(C, 26, 27, 28) QK(N, 16, KU, 100) H4 EX3(C, 29, 30, 31)
#define KXL(j) "ds_read_b32 v128, v134 offset:(" #j "+2)*128\n\t"
#define STEP_EVEN(j, P, W, H2, H3, H4) STEP(j, P, W, 32, 64, 104, 108, 112, 116, 124, 120, 1, "", "", KXL(j), H2, H3, H4)
#define STEP_ODD(j, P, W, H1, H2, H3, H4) STEP(j, P, W, 64, 32, 112, 116, 104, 108, 120, 124, 0, "", "", H1, H2, H3, H4)
#define DOT(d, a, c) "v_dot2_f32_bf16 v" #d ", v" #a ", v" #a ", " c "\n\t"
#define UNP(d, r) "v_mfma_f32_4x4x4_16b_bf16 v[" #d ":" #d "+3], v[172:173], v[" #r ":" #r "+1], 0\n\t"
#define K_NORM DOT(150, 136, "0") DOT(151, 137, "0") DOT(150, 138, "v150") DOT(151, 139, "v151") DOT(150, 140, "v150") \
    DOT(151, 141, "v151") DOT(150, 142, "v150") DOT(151, 143, "v151") DOT(150, 144, "v150") \
    UNP(152, 136) UNP(156, 138) UNP(160, 140) "v_add_f32 v150, v150, v151\n\tv_sqrt_f32 v150, v150\n\t" UNP(164, 142) \
    "v_add_f32 v150, 0x358637bd, v150\n\tv_rcp_f32_e64 v150, v150\n\t" UNP(168, 144)
#define PKM(x) "v_pk_mul_f32 v[" #x ":" #x "+1], v[150:151], v[" #x ":" #x "+1] op_sel_hi:[0,1]\n\t"
#define CVT(d, x) "v_cvt_pk_bf16_f32 v[" #d "], v[" #x "], v[" #x "+1]\n\t"
#define K_PK8(x) PKM(x) PKM(x + 2) PKM(x + 4) PKM(x + 6) CVT(x, x) CVT(x + 1, x + 2) CVT(x + 2, x + 4) CVT(x + 3, x + 6)
#define SKIP7(PART) "s_cbranch_scc1 .Lqk_skip_%=\n\t" NOP PART ".Lqk_skip_%=:\n\t"
#define WL(r, s) "ds_write_b16 v146, v" #r " offset:" #s "*16\n\t"
#define WH(r, s) "ds_write_b16_d16_hi v146, v" #r " offset:" #s "*16\n\t"
#define LDA "global_load_dwordx4 v[136:139], v149, s[20:21]\n\t"
#define LDB "global_load_dwordx4 v[140:143], v149, s[20:21] offset:16\n\t"
#define LDC "global_load_dword v144, v149, s[20:21] offset:32\n\t"
#define FLAG "ds_write_b32 v181, v179\n\t"
#define BODY_TOP(r) \
    ".p2align 6\n" \
    ".Lqk_top_" #r "_%=:\n\t" \
    STEP(0, 3, "", 32, 64, 104, 108, 112, 116, 124, 120, 1, "v_cmp_lt_u32_e64 vcc, v180, v179\n\t", \
         "s_cbranch_vccnz .Lqk_spin_" #r "_%=\n\t" NOP ".Lqk_go_" #r "_%=:\n\t", KXL(0), "", "", "") \
    "s_cmp_eq_u32 %[it], 8\n\ts_cbranch_scc1 .Lqk_end_%=\n\t" \
    STEP_ODD(1, 3, "", "", "v_add_u32_e64 v179, 1, v179\n\t", "", "") \
    ".Lqk_mid_" #r "_%=:\n\t" \
    "s_cmp_eq_u32 %[it], 7\n\t" NOP
#define BODY_END(r) \
    STEP_ODD(7, 0, "", "s_cmp_eq_u32 %[it], 6\n\ts_cselect_b32 %[tmp], -7, 1\n\t", "ds_read_b32 v180, v182\n\tv_mad_i32_i24 v146, %[tmp], v177, v146\n\t", \
             "v_mad_i32_i24 v147, %[tmp], v178, v147\n\t", "v_add_u32 v133, 0x2000, v133\n\t") \
    "v_add_u32 v134, 0x400, v134\n\tv_add_u32 v135, 0x2800, v135\n\t" \
    "s_add_u32 %[it], %[it], 1\n\ts_cmp_lt_u32 %[it], 6\n\ts_cselect_b32 %[tmp], 0x2400, 0\n\t"        \
    "s_add_u32 s20, s20, %[tmp]\n\ts_addc_u32 s21, s21, 0\n\t" \
    "s_branch .Lqk_top_" #r "_%=\n" \
    ".Lqk_spin_" #r "_%=:\n\ts_setprio 0\n\tds_read_b32 v180, v182\n\ts_waitcnt lgkmcnt(0)\n\tv_cmp_lt_u32 vcc, v180, v179\n\ts_nop 4\n\t" \
    "s_cbranch_vccnz .Lqk_spin_" #r "_%=\n\ts_setprio 3\n\ts_branch .Lqk_go_" #r "_%=\n\t"
#define BODY_K BODY_TOP(k) \
    STEP_EVEN(2, 3, "vmcnt(0) ", SKIP7(K_NORM K_PK8(152) K_PK8(160) PKM(168) CVT(168, 168)), \
              "ds_write_b128 v146, v[152:155]\n\t", "ds_write_b128 v146, v[160:163] offset:512\n\t") \
    STEP_ODD(3, 3, "", "ds_write_b32 v147, v168\n\t", FLAG, LDA, LDB) \
    STEP_EVEN(4, 2, "", LDC, "", "") \
    STEP_ODD(5, 2, "", "", "", "", "") \
    STEP_EVEN(6, 1, "", "", "", "") BODY_END(k)
#define BODY_V BODY_TOP(v) \
    STEP_EVEN(2, 3, "vmcnt(2) ", WL(136, 0), WH(136, 1) WL(137, 2), WH(137, 3)) \
    STEP_ODD(3, 3, "", WL(138, 4), WH(138, 5), WL(139, 6) WH(139, 7), LDA) \
    STEP_EVEN(4, 2, "vmcnt(2) ", WL(140, 8), WH(140, 9) WL(141, 10), WH(141, 19)) \
    STEP_ODD(5, 2, "", WL(142, 12), WH(142, 13), WL(143, 14) WH(143, 15), LDB) \
    STEP_EVEN(6, 1, "vmcnt(2) ", WL(144, 16), WH(144, 17) "ds_write_b32 v147, v148\n\t", FLAG LDC) BODY_END(v)
#define OMUL(O, i) "v_pk_mul_f32 v[" #O "+" #i ":" #O "+" #i "+1], v[" #O "+10:" #O "+11], v[" #O "+" #i ":" #O "+" #i "+1] op_sel_hi:[0,1]\n\t"
#define OCVT(O, d, i) "v_cvt_pk_bf16_f32 v[" #O "+" #d "], v[" #O "+" #i "], v[" #O "+" #i "+1]\n\t"
#define OUT_HEAD(O) \
    "v_mov_b32 v[" #O "+11], v[" #O "+10]\n\ts_nop 1\n\tv_permlane32_swap_b32 v[" #O "+10], v[" #O "+11]\n\tv_rcp_f32 v[" #O "+10], v[" #O "+10]\n\ts_nop 0\n\t"
#define OUT_REST(O, A) \
    OMUL(O, 0) OMUL(O, 2) OMUL(O, 4) OMUL(O, 6) OMUL(O, 8) OCVT(O, 0, 0) OCVT(O, 1, 2) OCVT(O, 2, 4) OCVT(O, 3, 6) OCVT(O, 4, 8) \
    "ds_write2_b32 v" #A ", v[" #O "], v[" #O "+1] offset1:1\n\tds_write2_b32 v" #A ", v[" #O "+2], v[" #O "+3] offset0:4 offset1:5\n\t" \
    "s_mov_b32 exec_hi, 0\n\tds_write_b32 v" #A ", v[" #O "+4] offset:32\n\ts_mov_b32 exec_hi, -1\n\t"
#define LDROW(a, b, c, off, base) \
    "global_load_dwordx4 " a ", " off ", " base "\n\tglobal_load_dwordx4 " b ", " off ", " base " offset:16\n\tglobal_load_dword " c ", " off ", " base " offset:32\n\t"

__global__ __launch_bounds__(512) void qk_norm_attn(const unsigned* __restrict__ q, const unsigned* __restrict__ k,
                                                    const unsigned* __restrict__ v, const unsigned* __restrict__ t,
                                                    unsigned* __restrict__ out  ) {
    __shared__ __attribute__((aligned(16))) char smem[LDS_BYTES];
    lds_ptr lds = (lds_ptr)smem;
    const int tid = threadIdx.x, lane = tid & 63, r32 = lane & 31, hi = lane >> 5;
    const int wid = __builtin_amdgcn_readfirstlane(tid >> 6);
    const int b = blockIdx.x;
    const int head = (b & 7) + 8 * (b >> 5), qc = (b >> 3) & 3;
    const size_t hbase = (size_t)head * SEQ * 9;
    const int qrow = qc * 512 + wid * 64 + r32;
    const bool is_k = wid < 4;
    const int srow = tid & 255;
    g_ptr src = uniform_ptr((is_k ? k : v) + hbase);
    g_ptr qsrc = uniform_ptr(q + hbase);
    g_ptr dst = uniform_ptr(out + hbase + (size_t)(qc * 512 + wid * 64) * 9);
    const unsigned tw = t[(head & (NHEADS - 1)) >> 1];
    unsigned soff = srow * 36;
    const unsigned qoffh = qrow * 36 + 16 * hi, qoffx = qrow * 36 + 1152 * hi;
    Row9 raw, nxt; u32x4 qa0, qa1; unsigned qb;
    asm volatile("global_load_dword %4, %[qx], %[qs] offset:32\n\tglobal_load_dwordx4 %3, %[qh], %[qs]\n\t"
                 "global_load_dwordx4 %5, %[qh], %[qs] offset:1152\n\t" LDROW("%0", "%1", "%2", "%[so]", "%[src]")
                 : "=&v"(raw.a), "=&v"(raw.b), "=&v"(raw.c), "=&v"(qa0), "=&v"(qb), "=&v"(qa1)
                 : [so] "v"(soff), [src] "s"(src), [qh] "v"(qoffh), [qx] "v"(qoffx), [qs] "s"(qsrc) : "memory");
    int sm_step = is_k ? 8192 : 10240, sx_step = is_k ? 1024 : 10240;
    const int vrow = V_OFF + (srow >> 3) * 320;
    lds_ptr sm = lds + (is_k ? KM_OFF + (srow >> 5) * 1024 + (srow & 31) * 16 : vrow + (srow & 7) * 2);
    lds_ptr sx = lds + (is_k ? KX_OFF + srow * 4 : vrow + ((srow & 4) ? 11 * 16 : 18 * 16) + (srow & 3) * 4);
    unsigned cval = (srow & 4) ? 0u : 0x3f803f80u;
    const float c = __uint_as_float((head & 1) ? (tw & 0xffff0000u) : (tw << 16)) * LOG2E;
    const bool fast = __builtin_fabsf(c) <= SHIFT_SAFE;
    const unsigned shift_bits = fast ? (cvt_pk(-__builtin_fabsf(c), 0.f) & 0xffffu) : 0u;
    const int vslot = r32 == 11 ? 19 : (r32 > 18 ? 11 : r32);
    lds_ptr kmp = lds + KM_OFF - 2048 + lane * 16;
    lds_ptr kxp = lds + KX_OFF - 256 + lane * 4;
    lds_ptr vp = lds + V_OFF - 2560 + hi * 320 + vslot * 16;
    int sel = 0xDDDD;
    lds_ptr patch = lds + V_OFF + 20480 + wid * 2304;
    lds_ptr orow = patch + r32 * 36 + 8 * hi, olin = patch + lane * 16, fown = lds + wid * 4, fall = lds + (lane & 7) * 4;
    u32x2 ident = (u32x2){(lane & 2) ? 0u : (0x3f80u << (16 * (lane & 1))), (lane & 2) ? (0x3f80u << (16 * (lane & 1))) : 0u};
    u32x2 kx = (u32x2){0u, ONE_ZERO};
    int token = 1, zero = 0, ooff = lane * 16;
    f32x16 o0, o1; u32x8 pz; u32x4 vz;
#pragma unroll
    for (int i = 0; i < 16; ++i) { o0[i] = 0.f; o1[i] = 0.f; }
#pragma unroll
    for (int i = 0; i < 8; ++i) { pz[i] = 0u; vz[i & 3] = 0u; }
    asm volatile("" : "+{v[0:15]}"(o0), "+{v[16:31]}"(o1), "+{v[88:95]}"(pz), "+{v[116:119]}"(vz), "+{v[128:129]}"(kx), "+{v132}"(sel),
                 "+{v133}"(kmp), "+{v134}"(kxp), "+{v135}"(vp), "+{v145}"(zero), "+{v148}"(cval), "+{v149}"(soff), "+{v[172:173]}"(ident),
                 "+{v174}"(orow), "+{v175}"(olin), "+{v176}"(ooff), "+{v177}"(sm_step), "+{v178}"(sx_step), "+{v179}"(token),
                 "+{v181}"(fown), "+{v182}"(fall), "+s"(dst));
    u32x4 qm0, qm1;
    asm volatile("s_waitcnt vmcnt(4)" : "+v"(qa0), "+v"(qb));
    const float g0 = prep_q(qa0, hi ? 0u : qb, c, qm0);
    asm volatile("s_waitcnt vmcnt(3)" : "+v"(qa1));
    const float g1 = prep_q(qa1, hi ? qb : 0u, c, qm1);
    u32x2 qx = (u32x2){scale2(qb, hi ? g1 : g0), shift_bits};
    PIN(qm0); PIN(qm1); PIN(qx);
    asm volatile("s_waitcnt vmcnt(0)\n\t" LDROW("v[136:139]", "v[140:143]", "v144", "v149", "%[src]")
                 : "=&{v[136:139]}"(nxt.a), "=&{v[140:143]}"(nxt.b), "=&{v144}"(nxt.c), "+v"(raw.a), "+v"(raw.b), "+v"(raw.c)
                 : "{v149}"(soff), [src] "s"(src + 9216) : "memory");
    if (is_k) stage_k(sm, sx, raw); else stage_v(sm, sx, cval, raw);
    sm += sm_step; sx += sx_step;
    *(LDS_AS(int)*)fown = 0;
    __syncthreads();

    if (__builtin_expect(fast, 1)) {
        int tmp, it = 0;
        f32x32 sa; u32x16 sb0; u32x8 sb1, vf0, kf; u32x4 vf1;
        unsigned long long nsrc = (unsigned long long)(src + 2 * 9216);
        asm volatile(
            "ds_read_b32 v128, v134 offset:256\n\t" LD128(120, "v133", "2048") LD128(124, "v133", "3072")
            LD128(104, "v135", "2560") LD128(108, "v135", "3200")
            "s_waitcnt lgkmcnt(4)\n\t" QX(32, 0) "s_nop 15\n\ts_nop 1\n\ts_waitcnt lgkmcnt(0)\n\t" QK(32, 0, 120, 96) QK(32, 16, 120, 100) "s_nop 7\n\t"
            "s_cmp_gt_u32 %[wid], 3\n\ts_cbranch_scc1 .Lqk_mid_v_%=\n\ts_branch .Lqk_mid_k_%=\n\t"
            BODY_K BODY_V
            ".p2align 5\n"
            ".Lqk_end_%=:\n\t"
            HD(0, "", 64) PV(16, 108, 32, 24) EX8(64, 2, 3, 4, 5, 6, 7, 8, 9) PV(0, 112, 64, 0) EX8(64, 10, 11, 12, 13, 14, 15, 16, 17)
            PV(0, 116, 64, 8) EX8(64, 18, 19, 20, 21, 22, 23, 24, 25) PV(16, 112, 64, 16) EX2(64, 26, 27) EX2(64, 28, 29) EX2(64, 30, 31)
            OUT_HEAD(0) PV(16, 116, 64, 24) OUT_REST(0, 174)
            "v_add_u32 v174, 1152, v174\n\t"
            OUT_HEAD(16) OUT_REST(16, 174)
            "s_waitcnt lgkmcnt(0)\n\t"
            "ds_read_b128 v[32:35], v175\n\tds_read_b128 v[36:39], v175 offset:1024\n\tds_read_b128 v[40:43], v175 offset:2048\n\t"
            "s_waitcnt lgkmcnt(2)\n\tglobal_store_dwordx4 v176, v[32:35], %[dst] sc1\n\t"
            "s_waitcnt lgkmcnt(1)\n\tglobal_store_dwordx4 v176, v[36:39], %[dst] offset:1024 sc1\n\t"
            "s_mov_b64 exec, 0xffff\n\t"
            "s_waitcnt lgkmcnt(0)\n\tglobal_store_dwordx4 v176, v[40:43], %[dst] offset:2048 sc1\n\t"
            "s_mov_b64 exec, -1\n\ts_nop 1"
            : "+{v[0:15]}"(o0), "+{v[16:31]}"(o1), "=&{v[32:63]}"(sa), "=&{v[64:79]}"(sb0), "=&{v[80:87]}"(sb1), "+{v[88:95]}"(pz),
              "=&{v[104:111]}"(vf0), "=&{v[112:115]}"(vf1), "+{v[116:119]}"(vz), "=&{v[120:127]}"(kf),
              "+{v[128:129]}"(kx), "+{v133}"(kmp), "+{v134}"(kxp), "+{v135}"(vp), "+{v[136:139]}"(nxt.a), "+{v[140:143]}"(nxt.b),
              "+{v144}"(nxt.c), "+{v146}"(sm), "+{v147}"(sx), "+{v174}"(orow), "+{v179}"(token), [it] "+s"(it), "+{s[20:21]}"(nsrc),
              [tmp] "=&s"(tmp)
            : "{v[96:99]}"(qm0), "{v[100:103]}"(qm1), "{v[130:131]}"(qx), "{v132}"(sel), "{v145}"(zero), "{v148}"(cval), "{v149}"(soff),
              "{v[172:173]}"(ident), "{v175}"(olin), "{v176}"(ooff), "{v177}"(sm_step), "{v178}"(sx_step),
              "{v181}"(fown), "{v182}"(fall), [dst] "s"(dst), [wid] "s"(wid)
            : "memory", "scc", "vcc", "v150", "v151", "v152", "v153", "v154", "v155", "v156", "v157", "v158", "v159", "v160", "v161",
              "v162", "v163", "v164", "v165", "v166", "v167", "v168", "v169", "v170", "v171", "v180");
    } else {
        asm volatile("s_waitcnt vmcnt(0)" :: "{v[136:139]}"(nxt.a), "{v[140:143]}"(nxt.b), "{v144}"(nxt.c) : "memory");
        float m0 = -INFINITY, m1 = -INFINITY;
        kmp += 2048; vp += 2560;
        kxp = lds + KX_OFF + r32 * 4;
        const bf16x8 q0 = __builtin_bit_cast(bf16x8, qm0), q1 = __builtin_bit_cast(bf16x8, qm1);
        const s16x4 qxs = __builtin_bit_cast(s16x4, qx);
#pragma unroll 1
        for (int i = 0; i < 64; ++i) {
            if ((i & 7) == 0 && i > 0) {
                raw = load_row(src + (i >> 3) * 9216, soff);
                if (is_k) stage_k(sm, sx, raw); else stage_v(sm, sx, cval, raw);
                sm += sm_step; sx += sx_step;
                __syncthreads();
            }
            const bf16x8 kf = LDB128(kmp), v0 = LDB128(vp), v1 = LDB128(vp + 640);
            const s16x4 kx = __builtin_bit_cast(s16x4, (u32x2){*(const LDS_AS(unsigned)*)kxp, ONE_ZERO});
            f32x32 zero32;
#pragma unroll
            for (int j = 0; j < 32; ++j) zero32[j] = 0.f;
            const f32x32 sx32 = __builtin_amdgcn_mfma_f32_32x32x4bf16_1k(kx, qxs, zero32, 0, 0, 0);
            f32x16 s0 = MF16(kf, q0, LO16(sx32)), s1 = MF16(kf, q1, HI16(sx32));
#define ONLINE(S, M, O) do { float mx = S[0]; _Pragma("unroll") for (int j = 1; j < 16; ++j) mx = __builtin_fmaxf(mx, S[j]); \
                auto sw = __builtin_amdgcn_permlane32_swap(__float_as_uint(mx), __float_as_uint(mx), false, false); \
                const float mn = __builtin_fmaxf(M, __builtin_fmaxf(__uint_as_float(sw[0]), __uint_as_float(sw[1]))); \
                const float alpha = __builtin_amdgcn_exp2f(M - mn); M = mn; \
                _Pragma("unroll") for (int j = 0; j < 16; ++j) { O[j] *= alpha; S[j] = __builtin_amdgcn_exp2f(S[j] - mn); } \
                O = SMF(v0, LO8(S), O); O = SMF(v1, HI8(S), O); } while (0)
            ONLINE(s0, m0, o0);
            ONLINE(s1, m1, o1);
            kmp += 1024; kxp += 128; vp += 1280;
        }
        put_o(patch + r32 * 36, o0, hi);
        put_o(patch + (r32 + 32) * 36, o1, hi);
        __builtin_amdgcn_fence(__ATOMIC_RELEASE, "wavefront");
        __builtin_amdgcn_wave_barrier();
        const u32x4 x0 = *(LDS_AS(u32x4)*)(patch + lane * 16), x1 = *(LDS_AS(u32x4)*)(patch + 1024 + lane * 16);
        const u32x4 x2 = *(LDS_AS(u32x4)*)(patch + 2048 + (lane & 15) * 16);
        store16(dst, lane * 16, x0);
        store16(dst, 1024 + lane * 16, x1);
        if (lane < 16) store16(dst, 2048 + lane * 16, x2);
        __builtin_amdgcn_s_waitcnt(0x0F70);
    }
}

extern "C" void kernel_launch(void* const* d_in, const int* in_sizes, int n_in, void* d_out, int out_size, void* d_ws,
                              size_t ws_size, hipStream_t stream) {
    qk_norm_attn<<<256, 512, 0, stream>>>((const unsigned*)d_in[0], (const unsigned*)d_in[1], (const unsigned*)d_in[2],
                                          (const unsigned*)d_in[3], (unsigned*)d_out  );
}
```

```cpp
#include <hip/hip_runtime.h>
#include <hip/hip_bf16.h>

typedef __attribute__((ext_vector_type(8))) __bf16 bf16x8;
typedef __attribute__((ext_vector_type(16))) __bf16 bf16x16;
typedef __attribute__((ext_vector_type(2))) __bf16 bf16x2;
typedef __attribute__((ext_vector_type(4))) short s16x4;
typedef __attribute__((ext_vector_type(2))) float f32x2;
typedef __attribute__((ext_vector_type(16))) float f32x16;
typedef __attribute__((ext_vector_type(32))) float f32x32;
typedef __attribute__((ext_vector_type(2))) unsigned u32x2;
typedef __attribute__((ext_vector_type(4))) unsigned u32x4;
typedef __attribute__((ext_vector_type(8))) unsigned u32x8;
typedef __attribute__((ext_vector_type(16))) unsigned u32x16;
typedef __attribute__((address_space(3))) char* lds_ptr;
typedef const __attribute__((address_space(1))) char* g_ptr;
#define G_AS(T) const __attribute__((address_space(1))) T
#define LDS_AS(T) __attribute__((address_space(3))) T
#define DI __device__ __forceinline__

constexpr int SEQ = 2048, NHEADS = 32;
constexpr int KX_OFF = 256;
constexpr int KM_OFF = KX_OFF + 8192;
constexpr int V_OFF = KM_OFF + 65536;
constexpr int LDS_BYTES = V_OFF + 256 * 320 + 1280;
constexpr float LOG2E = 1.4426950408889634f;
constexpr float SHIFT_SAFE = 40.0f;
constexpr unsigned ONE_ZERO = 0x00003f80u;

typedef u32x4 __attribute__((aligned(4))) u32x4_u;
struct Row9 { u32x4 a, b; unsigned c; };

DI Row9 load_row(g_ptr base, unsigned off) {
    Row9 r; g_ptr p = base + off;
    r.a = *(G_AS(u32x4_u)*)p; r.b = *(G_AS(u32x4_u)*)(p + 16); r.c = *(G_AS(unsigned)*)(p + 32);
    return r;
}
DI unsigned cvt_pk(float lo, float hi) { f32x2 f = {lo, hi}; return __builtin_bit_cast(unsigned, __builtin_convertvector(f, bf16x2)); }
DI float sq2(unsigned w, float acc) { return __builtin_amdgcn_fdot2_f32_bf16(__builtin_bit_cast(bf16x2, w), __builtin_bit_cast(bf16x2, w), acc, false); }
DI unsigned scale2(unsigned w, float g) { return cvt_pk(__uint_as_float(w << 16) * g, __uint_as_float(w & 0xffff0000u) * g); }
DI float inv_norm(float ss) { return __builtin_amdgcn_rcpf(__builtin_amdgcn_sqrtf(ss) + 1e-6f); }
DI g_ptr uniform_ptr(const void* p) {
    const unsigned long long a = (unsigned long long)p;
    const unsigned lo = __builtin_amdgcn_readfirstlane((unsigned)a), hi = __builtin_amdgcn_readfirstlane((unsigned)(a >> 32));
    return (g_ptr)(((unsigned long long)hi << 32) | lo);
}
DI void stage_k(lds_ptr pm, lds_ptr px, const Row9& r) {
    float s0 = sq2(r.a[0], 0.f), s1 = sq2(r.a[1], 0.f);
    s0 = sq2(r.a[2], s0); s1 = sq2(r.a[3], s1); s0 = sq2(r.b[0], s0); s1 = sq2(r.b[1], s1);
    s0 = sq2(r.b[2], s0); s1 = sq2(r.b[3], s1); s0 = sq2(r.c, s0);
    const float g = inv_norm(s0 + s1);
    *(LDS_AS(u32x4)*)pm = (u32x4){scale2(r.a[0], g), scale2(r.a[1], g), scale2(r.a[2], g), scale2(r.a[3], g)};
    *(LDS_AS(u32x4)*)(pm + 512) = (u32x4){scale2(r.b[0], g), scale2(r.b[1], g), scale2(r.b[2], g), scale2(r.b[3], g)};
    *(LDS_AS(unsigned)*)px = scale2(r.c, g);
}
DI void stage_v(lds_ptr pm, lds_ptr px, unsigned cval, const Row9& r) {
#define W2(w, s_lo, s_hi) *(LDS_AS(unsigned short)*)(pm + (s_lo) * 16) = (unsigned short)(w); *(LDS_AS(unsigned short)*)(pm + (s_hi) * 16) = (unsigned short)((w) >> 16)
    W2(r.a[0], 0, 1); W2(r.a[1], 2, 3); W2(r.a[2], 4, 5); W2(r.a[3], 6, 7);
    W2(r.b[0], 8, 9); W2(r.b[1], 10, 19); W2(r.b[2], 12, 13); W2(r.b[3], 14, 15);
    W2(r.c, 16, 17);
#undef W2
    *(LDS_AS(unsigned)*)px = cval;
}
DI float prep_q(const u32x4 a, const unsigned e, float c, u32x4& qm) {
    float s0 = sq2(a[0], 0.f), s1 = sq2(a[1], 0.f);
    s0 = sq2(a[2], s0); s1 = sq2(a[3], s1); s0 = sq2(e, s0);
    s0 += s1;
    auto sw = __builtin_amdgcn_permlane32_swap(__float_as_uint(s0), __float_as_uint(s0), false, false);
    const float g = c * inv_norm(__uint_as_float(sw[0]) + __uint_as_float(sw[1]));
    qm = (u32x4){scale2(a[0], g), scale2(a[1], g), scale2(a[2], g), scale2(a[3], g)};
    return g;
}
DI void put_o(lds_ptr p, const f32x16& o, int hi) {
    auto sw = __builtin_amdgcn_permlane32_swap(__float_as_uint(o[10]), __float_as_uint(o[10]), false, false);
    const float rl = __builtin_amdgcn_rcpf(__uint_as_float(sw[0]));
    LDS_AS(unsigned)* w = (LDS_AS(unsigned)*)(p + 8 * hi);
    w[0] = cvt_pk(o[0] * rl, o[1] * rl); w[1] = cvt_pk(o[2] * rl, o[3] * rl);
    w[4] = cvt_pk(o[4] * rl, o[5] * rl); w[5] = cvt_pk(o[6] * rl, o[7] * rl);
    if (!hi) w[8] = cvt_pk(o[8] * rl, o[9] * rl);
}
DI void store16(g_ptr base, unsigned off, u32x4 x) {
    asm volatile("global_store_dwordx4 %0, %1, %2 sc1\n\ts_nop 1" :: "v"(off), "v"(x), "s"(base) : "memory");
}
#define LO16(x) __builtin_shufflevector(x, x, 0, 1, 2, 3, 4, 5, 6, 7, 8, 9, 10, 11, 12, 13, 14, 15)
#define HI16(x) __builtin_shufflevector(x, x, 16, 17, 18, 19, 20, 21, 22, 23, 24, 25, 26, 27, 28, 29, 30, 31)
#define LO8(x) __builtin_bit_cast(bf16x16, __builtin_shufflevector(x, x, 0, 1, 2, 3, 4, 5, 6, 7))
#define HI8(x) __builtin_bit_cast(bf16x16, __builtin_shufflevector(x, x, 8, 9, 10, 11, 12, 13, 14, 15))
#define SMF(a, b, c) __builtin_amdgcn_smfmac_f32_32x32x32_bf16(a, b, c, sel, 0, 0)
#define MF16(a, b, c) __builtin_amdgcn_mfma_f32_32x32x16_bf16(a, b, c, 0, 0, 0)
#define PIN(x) asm volatile("" : "+v"(x))
#define LDB128(p) (*(const LDS_AS(bf16x8)*)(p))

#define EX(C, i) "v_exp_f32 v[" #C "+" #i "], v[" #C "+" #i "]\n\t"
#define EXL(C, i) "v_exp_f32_e64 v[" #C "+" #i "], v[" #C "+" #i "]\n\t"
#define EX2(C, a, b) EX(C, a) EX(C, b)
#define EX3(C, a, b, c) EX2(C, a, b) EXL(C, c)
#define EX5(C, a, b, c, d, e) EX2(C, a, b) EX3(C, c, d, e)
#define EX8(C, a, b, c, d, e, f, g, h) EX2(C, a, b) EX2(C, c, d) EX2(C, e, f) EX2(C, g, h)
#define PV(O, V, P, i) "v_smfmac_f32_32x32x32_bf16 v[" #O ":" #O "+15], v[" #V ":" #V "+3], v[" #P "+" #i ":" #P "+" #i "+7], v132\n\t"
#define QK(N, i, K, Q) "v_mfma_f32_32x32x16_bf16 v[" #N "+" #i ":" #N "+" #i "+15], v[" #K ":" #K "+3], v[" #Q ":" #Q "+3], v[" #N "+" #i ":" #N "+" #i "+15]\n\t"
#define QX(N, ab) "v_mfma_f32_32x32x4_2b_bf16 v[" #N ":" #N "+31], v[128:129], v[130:131], 0 cbsz:1 abid:" #ab "\n\t"
#define LD128(D, A, off) "ds_read_b128 v[" #D ":" #D "+3], " A " offset:" off "\n\t"
#define NOP "s_nop 0\n\t"
#define HD(p, w, C) "s_setprio " #p "\n\ts_waitcnt " w "lgkmcnt(0)\n\t" EX2(C, 0, 1)
#define STEP(j, P, W, C, N, VC0, VC1, VN0, VN1, KU, KL, ab, HA, HB, H1, H2, H3, H4) \
    HD(P, W, C) PV(16, VN1, N, 24) LD128(VN0, "v135", "(" #j "+1)*1280") EX3(C, 2, 3, 4) \
    QX(N, ab) LD128(VN1, "v135", "(" #j "+1)*1280+640") HA EX8(C, 5, 6, 7, 8, 9, 10, 11, 12) \
    HB PV(0, VC0, C, 0) LD128(KL, "v133", "(" #j "+2)*1024") EX5(C, 13, 14, 15, 16, 17) \
    PV(0, VC1, C, 8) H1 EX3(C, 18, 19, 20) \
    QK(N, 0, KU, 96) H2 EX5(C, 21, 22, 23, 24, 25) PV(16, VC0, C, 16) H3 EX3(C, 26, 27, 28) QK(N, 16, KU, 100) H4 EX3(C, 29, 30, 31)
#define KXL(j) "ds_read_b32 v128, v134 offset:(" #j "+2)*128\n\t"
#define STEP_EVEN(j, P, W, H2, H3, H4) STEP(j, P, W, 32, 64, 104, 108, 112, 116, 124, 120, 1, "", "", KXL(j), H2, H3, H4)
#define STEP_ODD(j, P, W, H1, H2, H3, H4) STEP(j, P, W, 64, 32, 112, 116, 104, 108, 120, 124, 0, "", "", H1, H2, H3, H4)
#define DOT(d, a, c) "v_dot2_f32_bf16 v" #d ", v" #a ", v" #a ", " c "\n\t"
#define UNP(d, r) "v_mfma_f32_4x4x4_16b_bf16 v[" #d ":" #d "+3], v[172:173], v[" #r ":" #r "+1], 0\n\t"
#define K_NORM DOT(150, 136, "0") DOT(151, 137, "0") DOT(150, 138, "v150") DOT(151, 139, "v151") DOT(150, 140, "v150") \
    DOT(151, 141, "v151") DOT(150, 142, "v150") DOT(151, 143, "v151") DOT(150, 144, "v150") \
    UNP(152, 136) UNP(156, 138) UNP(160, 140) "v_add_f32 v150, v150, v151\n\tv_sqrt_f32 v150, v150\n\t" UNP(164, 142) \
    "v_add_f32 v150, 0x358637bd, v150\n\tv_rcp_f32_e64 v150, v150\n\t" UNP(168, 144)
#define PKM(x) "v_pk_mul_f32 v[" #x ":" #x "+1], v[150:151], v[" #x ":" #x "+1] op_sel_hi:[0,1]\n\t"
#define CVT(d, x) "v_cvt_pk_bf16_f32 v[" #d "], v[" #x "], v[" #x "+1]\n\t"
#define K_PK8(x) PKM(x) PKM(x + 2) PKM(x + 4) PKM(x + 6) CVT(x, x) CVT(x + 1, x + 2) CVT(x + 2, x + 4) CVT(x + 3, x + 6)
#define SKIP7(PART) "s_cbranch_scc1 .Lqk_skip_%=\n\t" NOP PART ".Lqk_skip_%=:\n\t"
#define WL(r, s) "ds_write_b16 v146, v" #r " offset:" #s "*16\n\t"
#define WH(r, s) "ds_write_b16_d16_hi v146, v" #r " offset:" #s "*16\n\t"
#define LDA "global_load_dwordx4 v[136:139], v149, s[20:21]\n\t"
#define LDB "global_load_dwordx4 v[140:143], v149, s[20:21] offset:16\n\t"
#define LDC "global_load_dword v144, v149, s[20:21] offset:32\n\t"
#define FLAG "ds_write_b32 v181, v179\n\t"
#define BODY_TOP(r) \
    ".p2align 6\n" \
    ".Lqk_top_" #r "_%=:\n\t" \
    STEP(0, 3, "", 32, 64, 104, 108, 112, 116, 124, 120, 1, "v_cmp_lt_u32_e64 vcc, v180, v179\n\t", \
         "s_cbranch_vccnz .Lqk_spin_" #r "_%=\n\t" NOP ".Lqk_go_" #r "_%=:\n\t", KXL(0), "", "", "") \
    "s_cmp_eq_u32 %[it], 8\n\ts_cbranch_scc1 .Lqk_end_%=\n\t" \
    STEP_ODD(1, 3, "", "", "v_add_u32_e64 v179, 1, v179\n\t", "", "") \
    ".Lqk_mid_" #r "_%=:\n\t" \
    "s_cmp_eq_u32 %[it], 7\n\t" NOP
#define BODY_END(r) \
    STEP_ODD(7, 0, "", "s_cmp_eq_u32 %[it], 6\n\ts_cselect_b32 %[tmp], -7, 1\n\t", "ds_read_b32 v180, v182\n\tv_mad_i32_i24 v146, %[tmp], v177, v146\n\t", \
             "v_mad_i32_i24 v147, %[tmp], v178, v147\n\t", "v_add_u32 v133, 0x2000, v133\n\t") \
    "v_add_u32 v134, 0x400, v134\n\tv_add_u32 v135, 0x2800, v135\n\t" \
    "s_add_u32 %[it], %[it], 1\n\ts_cmp_lt_u32 %[it], 6\n\ts_cselect_b32 %[tmp], 0x2400, 0\n\t"        \
    "s_add_u32 s20, s20, %[tmp]\n\ts_addc_u32 s21, s21, 0\n\t" \
    "s_branch .Lqk_top_" #r "_%=\n" \
    ".Lqk_spin_" #r "_%=:\n\ts_setprio 0\n\tds_read_b32 v180, v182\n\ts_waitcnt lgkmcnt(0)\n\tv_cmp_lt_u32 vcc, v180, v179\n\ts_nop 4\n\t" \
    "s_cbranch_vccnz .Lqk_spin_" #r "_%=\n\ts_setprio 3\n\ts_branch .Lqk_go_" #r "_%=\n\t"
#define BODY_K BODY_TOP(k) \
    STEP_EVEN(2, 3, "vmcnt(0) ", SKIP7(K_NORM K_PK8(152) K_PK8(160) PKM(168) CVT(168, 168)), \
              "ds_write_b128 v146, v[152:155]\n\t", "ds_write_b128 v146, v[160:163] offset:512\n\t") \
    STEP_ODD(3, 3, "", "ds_write_b32 v147, v168\n\t", FLAG, LDA, LDB) \
    STEP_EVEN(4, 2, "", LDC, "", "") \
    STEP_ODD(5, 2, "", "", "", "", "") \
    STEP_EVEN(6, 1, "", "", "", "") BODY_END(k)
#define BODY_V BODY_TOP(v) \
    STEP_EVEN(2, 3, "vmcnt(2) ", WL(136, 0), WH(136, 1) WL(137, 2), WH(137, 3)) \
    STEP_ODD(3, 3, "", WL(138, 4), WH(138, 5), WL(139, 6) WH(139, 7), LDA) \
    STEP_EVEN(4, 2, "vmcnt(2) ", WL(140, 8), WH(140, 9) WL(141, 10), WH(141, 19)) \
    STEP_ODD(5, 2, "", WL(142, 12), WH(142, 13), WL(143, 14) WH(143, 15), LDB) \
    STEP_EVEN(6, 1, "vmcnt(2) ", WL(144, 16), WH(144, 17) "ds_write_b32 v147, v148\n\t", FLAG LDC) BODY_END(v)
#define OMUL(O, i) "v_pk_mul_f32 v[" #O "+" #i ":" #O "+" #i "+1], v[" #O "+10:" #O "+11], v[" #O "+" #i ":" #O "+" #i "+1] op_sel_hi:[0,1]\n\t"
#define OCVT(O, d, i) "v_cvt_pk_bf16_f32 v[" #O "+" #d "], v[" #O "+" #i "], v[" #O "+" #i "+1]\n\t"
#define OUT_HEAD(O) \
    "v_mov_b32 v[" #O "+11], v[" #O "+10]\n\ts_nop 1\n\tv_permlane32_swap_b32 v[" #O "+10], v[" #O "+11]\n\tv_rcp_f32 v[" #O "+10], v[" #O "+10]\n\ts_nop 0\n\t"
#define OUT_REST(O, A) \
    OMUL(O, 0) OMUL(O, 2) OMUL(O, 4) OMUL(O, 6) OMUL(O, 8) OCVT(O, 0, 0) OCVT(O, 1, 2) OCVT(O, 2, 4) OCVT(O, 3, 6) OCVT(O, 4, 8) \
    "ds_write2_b32 v" #A ", v[" #O "], v[" #O "+1] offset1:1\n\tds_write2_b32 v" #A ", v[" #O "+2], v[" #O "+3] offset0:4 offset1:5\n\t" \
    "s_mov_b32 exec_hi, 0\n\tds_write_b32 v" #A ", v[" #O "+4] offset:32\n\ts_mov_b32 exec_hi, -1\n\t"
#define LDROW(a, b, c, off, base) \
    "global_load_dwordx4 " a ", " off ", " base "\n\tglobal_load_dwordx4 " b ", " off ", " base " offset:16\n\tglobal_load_dword " c ", " off ", " base " offset:32\n\t"

__global__ __launch_bounds__(512) void qk_norm_attn(const unsigned* __restrict__ q, const unsigned* __restrict__ k,
                                                    const unsigned* __restrict__ v, const unsigned* __restrict__ t,
                                                    unsigned* __restrict__ out  ) {
    __shared__ __attribute__((aligned(16))) char smem[LDS_BYTES];
    lds_ptr lds = (lds_ptr)smem;
    const int tid = threadIdx.x, lane = tid & 63, r32 = lane & 31, hi = lane >> 5;
    const int wid = __builtin_amdgcn_readfirstlane(tid >> 6);
    const int b = blockIdx.x;
    const int head = (b & 7) + 8 * (b >> 5), qc = (b >> 3) & 3;
    const size_t hbase = (size_t)head * SEQ * 9;
    const int qrow = qc * 512 + wid * 64 + r32;
    const bool is_k = wid < 4;
    const int srow = tid & 255;
    g_ptr src = uniform_ptr((is_k ? k : v) + hbase);
    g_ptr qsrc = uniform_ptr(q + hbase);
    g_ptr dst = uniform_ptr(out + hbase + (size_t)(qc * 512 + wid * 64) * 9);
    const unsigned tw = t[(head & (NHEADS - 1)) >> 1];
    unsigned soff = srow * 36;
    const unsigned qoffh = qrow * 36 + 16 * hi, qoffx = qrow * 36 + 1152 * hi;
    Row9 raw, nxt; u32x4 qa0, qa1; unsigned qb;
    asm volatile("global_load_dword %4, %[qx], %[qs] offset:32\n\tglobal_load_dwordx4 %3, %[qh], %[qs]\n\t"
                 "global_load_dwordx4 %5, %[qh], %[qs] offset:1152\n\t" LDROW("%0", "%1", "%2", "%[so]", "%[src]") LDROW("v[136:139]", "v[140:143]", "v144", "%[so]", "%[src1]")
                 : "=&v"(raw.a), "=&v"(raw.b), "=&v"(raw.c), "=&v"(qa0), "=&v"(qb), "=&v"(qa1),
                   "=&{v[136:139]}"(nxt.a), "=&{v[140:143]}"(nxt.b), "=&{v144}"(nxt.c)
                 : [so] "v"(soff), [src] "s"(src), [src1] "s"(src + 9216), [qh] "v"(qoffh), [qx] "v"(qoffx), [qs] "s"(qsrc) : "memory");
    int sm_step = is_k ? 8192 : 10240, sx_step = is_k ? 1024 : 10240;
    const int vrow = V_OFF + (srow >> 3) * 320;
    lds_ptr sm = lds + (is_k ? KM_OFF + (srow >> 5) * 1024 + (srow & 31) * 16 : vrow + (srow & 7) * 2);
    lds_ptr sx = lds + (is_k ? KX_OFF + srow * 4 : vrow + ((srow & 4) ? 11 * 16 : 18 * 16) + (srow & 3) * 4);
    unsigned cval = (srow & 4) ? 0u : 0x3f803f80u;
    const float c = __uint_as_float((head & 1) ? (tw & 0xffff0000u) : (tw << 16)) * LOG2E;
    const bool fast = __builtin_fabsf(c) <= SHIFT_SAFE;
    const unsigned shift_bits = fast ? (cvt_pk(-__builtin_fabsf(c), 0.f) & 0xffffu) : 0u;
    const int vslot = r32 == 11 ? 19 : (r32 > 18 ? 11 : r32);
    lds_ptr kmp = lds + KM_OFF - 2048 + lane * 16;
    lds_ptr kxp = lds + KX_OFF - 256 + lane * 4;
    lds_ptr vp = lds + V_OFF - 2560 + hi * 320 + vslot * 16;
    int sel = 0xDDDD;
    lds_ptr patch = lds + V_OFF + 20480 + wid * 2304;
    lds_ptr orow = patch + r32 * 36 + 8 * hi, olin = patch + lane * 16, fown = lds + wid * 4, fall = lds + (lane & 7) * 4;
    u32x2 ident = (u32x2){(lane & 2) ? 0u : (0x3f80u << (16 * (lane & 1))), (lane & 2) ? (0x3f80u << (16 * (lane & 1))) : 0u};
    u32x2 kx = (u32x2){0u, ONE_ZERO};
    int token = 1, zero = 0, ooff = lane * 16;
    f32x16 o0, o1; u32x8 pz; u32x4 vz;
#pragma unroll
    for (int i = 0; i < 16; ++i) { o0[i] = 0.f; o1[i] = 0.f; }
#pragma unroll
    for (int i = 0; i < 8; ++i) { pz[i] = 0u; vz[i & 3] = 0u; }
    asm volatile("" : "+{v[0:15]}"(o0), "+{v[16:31]}"(o1), "+{v[88:95]}"(pz), "+{v[116:119]}"(vz), "+{v[128:129]}"(kx), "+{v132}"(sel),
                 "+{v133}"(kmp), "+{v134}"(kxp), "+{v135}"(vp), "+{v145}"(zero), "+{v148}"(cval), "+{v149}"(soff), "+{v[172:173]}"(ident),
                 "+{v174}"(orow), "+{v175}"(olin), "+{v176}"(ooff), "+{v177}"(sm_step), "+{v178}"(sx_step), "+{v179}"(token),
                 "+{v181}"(fown), "+{v182}"(fall), "+s"(dst));
    u32x4 qm0, qm1;
    asm volatile("s_waitcnt vmcnt(7)" : "+v"(qa0), "+v"(qb));
    const float g0 = prep_q(qa0, hi ? 0u : qb, c, qm0);
    asm volatile("s_waitcnt vmcnt(6)" : "+v"(qa1));
    const float g1 = prep_q(qa1, hi ? qb : 0u, c, qm1);
    u32x2 qx = (u32x2){scale2(qb, hi ? g1 : g0), shift_bits};
    PIN(qm0); PIN(qm1); PIN(qx);
    asm volatile("s_waitcnt vmcnt(3)" : "+v"(raw.a), "+v"(raw.b), "+v"(raw.c));
    if (is_k) stage_k(sm, sx, raw); else stage_v(sm, sx, cval, raw);
    sm += sm_step; sx += sx_step;
    *(LDS_AS(int)*)fown = 0;
    __syncthreads();

    if (__builtin_expect(fast, 1)) {
        int tmp, it = 0;
        f32x32 sa; u32x16 sb0; u32x8 sb1, vf0, kf; u32x4 vf1;
        unsigned long long nsrc = (unsigned long long)(src + 2 * 9216);
        asm volatile(
            "ds_read_b32 v128, v134 offset:256\n\t" LD128(120, "v133", "2048") LD128(124, "v133", "3072")
            LD128(104, "v135", "2560") LD128(108, "v135", "3200")
            "s_waitcnt lgkmcnt(4)\n\t" QX(32, 0) "s_nop 15\n\ts_nop 1\n\ts_waitcnt lgkmcnt(0)\n\t" QK(32, 0, 120, 96) QK(32, 16, 120, 100) "s_nop 7\n\t"
            "s_cmp_gt_u32 %[wid], 3\n\ts_cbranch_scc1 .Lqk_mid_v_%=\n\ts_branch .Lqk_mid_k_%=\n\t"
            BODY_K BODY_V
            ".p2align 5\n"
            ".Lqk_end_%=:\n\t"
            HD(0, "", 64) PV(16, 108, 32, 24) EX8(64, 2, 3, 4, 5, 6, 7, 8, 9) PV(0, 112, 64, 0) EX8(64, 10, 11, 12, 13, 14, 15, 16, 17)
            PV(0, 116, 64, 8) EX8(64, 18, 19, 20, 21, 22, 23, 24, 25) PV(16, 112, 64, 16) EX2(64, 26, 27) EX2(64, 28, 29) EX2(64, 30, 31)
            OUT_HEAD(0) PV(16, 116, 64, 24) OUT_REST(0, 174)
            "v_add_u32 v174, 1152, v174\n\t"
            OUT_HEAD(16) OUT_REST(16, 174)
            "s_waitcnt lgkmcnt(0)\n\t"
            "ds_read_b128 v[32:35], v175\n\tds_read_b128 v[36:39], v175 offset:1024\n\tds_read_b128 v[40:43], v175 offset:2048\n\t"
            "s_waitcnt lgkmcnt(2)\n\tglobal_store_dwordx4 v176, v[32:35], %[dst] sc1\n\t"
            "s_waitcnt lgkmcnt(1)\n\tglobal_store_dwordx4 v176, v[36:39], %[dst] offset:1024 sc1\n\t"
            "s_mov_b64 exec, 0xffff\n\t"
            "s_waitcnt lgkmcnt(0)\n\tglobal_store_dwordx4 v176, v[40:43], %[dst] offset:2048 sc1\n\t"
            "s_mov_b64 exec, -1\n\ts_nop 1"
            : "+{v[0:15]}"(o0), "+{v[16:31]}"(o1), "=&{v[32:63]}"(sa), "=&{v[64:79]}"(sb0), "=&{v[80:87]}"(sb1), "+{v[88:95]}"(pz),
              "=&{v[104:111]}"(vf0), "=&{v[112:115]}"(vf1), "+{v[116:119]}"(vz), "=&{v[120:127]}"(kf),
              "+{v[128:129]}"(kx), "+{v133}"(kmp), "+{v134}"(kxp), "+{v135}"(vp), "+{v[136:139]}"(nxt.a), "+{v[140:143]}"(nxt.b),
              "+{v144}"(nxt.c), "+{v146}"(sm), "+{v147}"(sx), "+{v174}"(orow), "+{v179}"(token), [it] "+s"(it), "+{s[20:21]}"(nsrc),
              [tmp] "=&s"(tmp)
            : "{v[96:99]}"(qm0), "{v[100:103]}"(qm1), "{v[130:131]}"(qx), "{v132}"(sel), "{v145}"(zero), "{v148}"(cval), "{v149}"(soff),
              "{v[172:173]}"(ident), "{v175}"(olin), "{v176}"(ooff), "{v177}"(sm_step), "{v178}"(sx_step),
              "{v181}"(fown), "{v182}"(fall), [dst] "s"(dst), [wid] "s"(wid)
            : "memory", "scc", "vcc", "v150", "v151", "v152", "v153", "v154", "v155", "v156", "v157", "v158", "v159", "v160", "v161",
              "v162", "v163", "v164", "v165", "v166", "v167", "v168", "v169", "v170", "v171", "v180");
    } else {
        asm volatile("s_waitcnt vmcnt(0)" :: "{v[136:139]}"(nxt.a), "{v[140:143]}"(nxt.b), "{v144}"(nxt.c) : "memory");
        float m0 = -INFINITY, m1 = -INFINITY;
        kmp += 2048; vp += 2560;
        kxp = lds + KX_OFF + r32 * 4;
        const bf16x8 q0 = __builtin_bit_cast(bf16x8, qm0), q1 = __builtin_bit_cast(bf16x8, qm1);
        const s16x4 qxs = __builtin_bit_cast(s16x4, qx);
#pragma unroll 1
        for (int i = 0; i < 64; ++i) {
            if ((i & 7) == 0 && i > 0) {
                raw = load_row(src + (i >> 3) * 9216, soff);
                if (is_k) stage_k(sm, sx, raw); else stage_v(sm, sx, cval, raw);
                sm += sm_step; sx += sx_step;
                __syncthreads();
            }
            const bf16x8 kf = LDB128(kmp), v0 = LDB128(vp), v1 = LDB128(vp + 640);
            const s16x4 kx = __builtin_bit_cast(s16x4, (u32x2){*(const LDS_AS(unsigned)*)kxp, ONE_ZERO});
            f32x32 zero32;
#pragma unroll
            for (int j = 0; j < 32; ++j) zero32[j] = 0.f;
            const f32x32 sx32 = __builtin_amdgcn_mfma_f32_32x32x4bf16_1k(kx, qxs, zero32, 0, 0, 0);
            f32x16 s0 = MF16(kf, q0, LO16(sx32)), s1 = MF16(kf, q1, HI16(sx32));
#define ONLINE(S, M, O) do { float mx = S[0]; _Pragma("unroll") for (int j = 1; j < 16; ++j) mx = __builtin_fmaxf(mx, S[j]); \
                auto sw = __builtin_amdgcn_permlane32_swap(__float_as_uint(mx), __float_as_uint(mx), false, false); \
                const float mn = __builtin_fmaxf(M, __builtin_fmaxf(__uint_as_float(sw[0]), __uint_as_float(sw[1]))); \
                const float alpha = __builtin_amdgcn_exp2f(M - mn); M = mn; \
                _Pragma("unroll") for (int j = 0; j < 16; ++j) { O[j] *= alpha; S[j] = __builtin_amdgcn_exp2f(S[j] - mn); } \
                O = SMF(v0, LO8(S), O); O = SMF(v1, HI8(S), O); } while (0)
            ONLINE(s0, m0, o0);
            ONLINE(s1, m1, o1);
            kmp += 1024; kxp += 128; vp += 1280;
        }
        put_o(patch + r32 * 36, o0, hi);
        put_o(patch + (r32 + 32) * 36, o1, hi);
        __builtin_amdgcn_fence(__ATOMIC_RELEASE, "wavefront");
        __builtin_amdgcn_wave_barrier();
        const u32x4 x0 = *(LDS_AS(u32x4)*)(patch + lane * 16), x1 = *(LDS_AS(u32x4)*)(patch + 1024 + lane * 16);
        const u32x4 x2 = *(LDS_AS(u32x4)*)(patch + 2048 + (lane & 15) * 16);
        store16(dst, lane * 16, x0);
        store16(dst, 1024 + lane * 16, x1);
        if (lane < 16) store16(dst, 2048 + lane * 16, x2);
        __builtin_amdgcn_s_waitcnt(0x0F70);
    }
}

extern "C" void kernel_launch(void* const* d_in, const int* in_sizes, int n_in, void* d_out, int out_size, void* d_ws,
                              size_t ws_size, hipStream_t stream) {
    qk_norm_attn<<<256, 512, 0, stream>>>((const unsigned*)d_in[0], (const unsigned*)d_in[1], (const unsigned*)d_in[2],
                                          (const unsigned*)d_in[3], (unsigned*)d_out  );
}
```

```cpp
#include <hip/hip_runtime.h>
#include <hip/hip_bf16.h>

typedef __attribute__((ext_vector_type(8))) __bf16 bf16x8;
typedef __attribute__((ext_vector_type(16))) __bf16 bf16x16;
typedef __attribute__((ext_vector_type(2))) __bf16 bf16x2;
typedef __attribute__((ext_vector_type(4))) short s16x4;
typedef __attribute__((ext_vector_type(2))) float f32x2;
typedef __attribute__((ext_vector_type(16))) float f32x16;
typedef __attribute__((ext_vector_type(32))) float f32x32;
typedef __attribute__((ext_vector_type(2))) unsigned u32x2;
typedef __attribute__((ext_vector_type(4))) unsigned u32x4;
typedef __attribute__((ext_vector_type(8))) unsigned u32x8;
typedef __attribute__((ext_vector_type(16))) unsigned u32x16;
typedef __attribute__((address_space(3))) char* lds_ptr;
typedef const __attribute__((address_space(1))) char* g_ptr;
#define G_AS(T) const __attribute__((address_space(1))) T
#define LDS_AS(T) __attribute__((address_space(3))) T
#define DI __device__ __forceinline__

constexpr int SEQ = 2048, NHEADS = 32;
constexpr int KX_OFF = 256;
constexpr int KM_OFF = KX_OFF + 8192;
constexpr int V_OFF = KM_OFF + 65536;
constexpr int LDS_BYTES = V_OFF + 256 * 320 + 1280;
constexpr float LOG2E = 1.4426950408889634f;
constexpr float SHIFT_SAFE = 40.0f;
constexpr unsigned ONE_ZERO = 0x00003f80u;

typedef u32x4 __attribute__((aligned(4))) u32x4_u;
struct Row9 { u32x4 a, b; unsigned c; };

DI Row9 load_row(g_ptr base, unsigned off) {
    Row9 r; g_ptr p = base + off;
    r.a = *(G_AS(u32x4_u)*)p; r.b = *(G_AS(u32x4_u)*)(p + 16); r.c = *(G_AS(unsigned)*)(p + 32);
    return r;
}
DI unsigned cvt_pk(float lo, float hi) { f32x2 f = {lo, hi}; return __builtin_bit_cast(unsigned, __builtin_convertvector(f, bf16x2)); }
DI float sq2(unsigned w, float acc) { return __builtin_amdgcn_fdot2_f32_bf16(__builtin_bit_cast(bf16x2, w), __builtin_bit_cast(bf16x2, w), acc, false); }
DI unsigned scale2(unsigned w, float g) { return cvt_pk(__uint_as_float(w << 16) * g, __uint_as_float(w & 0xffff0000u) * g); }
DI float inv_norm(float ss) { return __builtin_amdgcn_rcpf(__builtin_amdgcn_sqrtf(ss) + 1e-6f); }
DI g_ptr uniform_ptr(const void* p) {
    const unsigned long long a = (unsigned long long)p;
    const unsigned lo = __builtin_amdgcn_readfirstlane((unsigned)a), hi = __builtin_amdgcn_readfirstlane((unsigned)(a >> 32));
    return (g_ptr)(((unsigned long long)hi << 32) | lo);
}
DI void stage_k(lds_ptr pm, lds_ptr px, const Row9& r) {
    float s0 = sq2(r.a[0], 0.f), s1 = sq2(r.a[1], 0.f);
    s0 = sq2(r.a[2], s0); s1 = sq2(r.a[3], s1); s0 = sq2(r.b[0], s0); s1 = sq2(r.b[1], s1);
    s0 = sq2(r.b[2], s0); s1 = sq2(r.b[3], s1); s0 = sq2(r.c, s0);
    const float g = inv_norm(s0 + s1);
    *(LDS_AS(u32x4)*)pm = (u32x4){scale2(r.a[0], g), scale2(r.a[1], g), scale2(r.a[2], g), scale2(r.a[3], g)};
    *(LDS_AS(u32x4)*)(pm + 512) = (u32x4){scale2(r.b[0], g), scale2(r.b[1], g), scale2(r.b[2], g), scale2(r.b[3], g)};
    *(LDS_AS(unsigned)*)px = scale2(r.c, g);
}
DI void stage_v(lds_ptr pm, lds_ptr px, unsigned cval, const Row9& r) {
#define W2(w, s_lo, s_hi) *(LDS_AS(unsigned short)*)(pm + (s_lo) * 16) = (unsigned short)(w); *(LDS_AS(unsigned short)*)(pm + (s_hi) * 16) = (unsigned short)((w) >> 16)
    W2(r.a[0], 0, 1); W2(r.a[1], 2, 3); W2(r.a[2], 4, 5); W2(r.a[3], 6, 7);
    W2(r.b[0], 8, 9); W2(r.b[1], 10, 19); W2(r.b[2], 12, 13); W2(r.b[3], 14, 15);
    W2(r.c, 16, 17);
#undef W2
    *(LDS_AS(unsigned)*)px = cval;
}
DI float prep_q(const u32x4 a, const unsigned e, float c, u32x4& qm) {
    float s0 = sq2(a[0], 0.f), s1 = sq2(a[1], 0.f);
    s0 = sq2(a[2], s0); s1 = sq2(a[3], s1); s0 = sq2(e, s0);
    s0 += s1;
    auto sw = __builtin_amdgcn_permlane32_swap(__float_as_uint(s0), __float_as_uint(s0), false, false);
    const float g = c * inv_norm(__uint_as_float(sw[0]) + __uint_as_float(sw[1]));
    qm = (u32x4){scale2(a[0], g), scale2(a[1], g), scale2(a[2], g), scale2(a[3], g)};
    return g;
}
DI void put_o(lds_ptr p, const f32x16& o, int hi) {
    auto sw = __builtin_amdgcn_permlane32_swap(__float_as_uint(o[10]), __float_as_uint(o[10]), false, false);
    const float rl = __builtin_amdgcn_rcpf(__uint_as_float(sw[0]));
    LDS_AS(unsigned)* w = (LDS_AS(unsigned)*)(p + 8 * hi);
    w[0] = cvt_pk(o[0] * rl, o[1] * rl); w[1] = cvt_pk(o[2] * rl, o[3] * rl);
    w[4] = cvt_pk(o[4] * rl, o[5] * rl); w[5] = cvt_pk(o[6] * rl, o[7] * rl);
    if (!hi) w[8] = cvt_pk(o[8] * rl, o[9] * rl);
}
DI void store16(g_ptr base, unsigned off, u32x4 x) {
    asm volatile("global_store_dwordx4 %0, %1, %2 sc1\n\ts_nop 1" :: "v"(off), "v"(x), "s"(base) : "memory");
}
#define LO16(x) __builtin_shufflevector(x, x, 0, 1, 2, 3, 4, 5, 6, 7, 8, 9, 10, 11, 12, 13, 14, 15)
#define HI16(x) __builtin_shufflevector(x, x, 16, 17, 18, 19, 20, 21, 22, 23, 24, 25, 26, 27, 28, 29, 30, 31)
#define LO8(x) __builtin_bit_cast(bf16x16, __builtin_shufflevector(x, x, 0, 1, 2, 3, 4, 5, 6, 7))
#define HI8(x) __builtin_bit_cast(bf16x16, __builtin_shufflevector(x, x, 8, 9, 10, 11, 12, 13, 14, 15))
#define SMF(a, b, c) __builtin_amdgcn_smfmac_f32_32x32x32_bf16(a, b, c, sel, 0, 0)
#define MF16(a, b, c) __builtin_amdgcn_mfma_f32_32x32x16_bf16(a, b, c, 0, 0, 0)
#define PIN(x) asm volatile("" : "+v"(x))
#define LDB128(p) (*(const LDS_AS(bf16x8)*)(p))

#define EX(C, i) "v_exp_f32 v[" #C "+" #i "], v[" #C "+" #i "]\n\t"
#define EXL(C, i) "v_exp_f32_e64 v[" #C "+" #i "], v[" #C "+" #i "]\n\t"
#define EX2(C, a, b) EX(C, a) EX(C, b)
#define EX3(C, a, b, c) EX2(C, a, b) EXL(C, c)
#define EX5(C, a, b, c, d, e) EX2(C, a, b) EX3(C, c, d, e)
#define EX8(C, a, b, c, d, e, f, g, h) EX2(C, a, b) EX2(C, c, d) EX2(C, e, f) EX2(C, g, h)
#define PV(O, V, P, i) "v_smfmac_f32_32x32x32_bf16 v[" #O ":" #O "+15], v[" #V ":" #V "+3], v[" #P "+" #i ":" #P "+" #i "+7], v132\n\t"
#define QK(N, i, K, Q) "v_mfma_f32_32x32x16_bf16 v[" #N "+" #i ":" #N "+" #i "+15], v[" #K ":" #K "+3], v[" #Q ":" #Q "+3], v[" #N "+" #i ":" #N "+" #i "+15]\n\t"
#define QX(N, ab) "v_mfma_f32_32x32x4_2b_bf16 v[" #N ":" #N "+31], v[128:129], v[130:131], 0 cbsz:1 abid:" #ab "\n\t"
#define LD128(D, A, off) "ds_read_b128 v[" #D ":" #D "+3], " A " offset:" off "\n\t"
#define NOP "s_nop 0\n\t"
#define HD(p, w, C) "s_setprio " #p "\n\ts_waitcnt " w "lgkmcnt(0)\n\t" EX2(C, 0, 1)
#define STEP(j, P, W, C, N, VC0, VC1, VN0, VN1, KU, KL, ab, HA, HB, H1, H2, H3, H4) \
    HD(P, W, C) PV(16, VN1, N, 24) LD128(VN0, "v135", "(" #j "+1)*1280") EX3(C, 2, 3, 4) \
    QX(N, ab) LD128(VN1, "v135", "(" #j "+1)*1280+640") HA EX8(C, 5, 6, 7, 8, 9, 10, 11, 12) \
    HB PV(0, VC0, C, 0) LD128(KL, "v133", "(" #j "+2)*1024") EX5(C, 13, 14, 15, 16, 17) \
    PV(0, VC1, C, 8) H1 EX3(C, 18, 19, 20) \
    QK(N, 0, KU, 96) H2 EX5(C, 21, 22, 23, 24, 25) PV(16, VC0, C, 16) H3 EX3(C, 26, 27, 28) QK(N, 16, KU, 100) H4 EX3(C, 29, 30, 31)
#define KXL(j) "ds_read_b32 v128, v134 offset:(" #j "+2)*128\n\t"
#define STEP_EVEN(j, P, W, H2, H3, H4) STEP(j, P, W, 32, 64, 104, 108, 112, 116, 124, 120, 1, "", "", KXL(j), H2, H3, H4)
#define STEP_ODD(j, P, W, H1, H2, H3, H4) STEP(j, P, W, 64, 32, 112, 116, 104, 108, 120, 124, 0, "", "", H1, H2, H3, H4)
#define DOT(d, a, c) "v_dot2_f32_bf16 v" #d ", v" #a ", v" #a ", " c "\n\t"
#define UNP(d, r) "v_mfma_f32_4x4x4_16b_bf16 v[" #d ":" #d "+3], v[172:173], v[" #r ":" #r "+1], 0\n\t"
#define K_NORM DOT(150, 136, "0") DOT(151, 137, "0") DOT(150, 138, "v150") DOT(151, 139, "v151") DOT(150, 140, "v150") \
    DOT(151, 141, "v151") DOT(150, 142, "v150") DOT(151, 143, "v151") DOT(150, 144, "v150") \
    UNP(152, 136) UNP(156, 138) UNP(160, 140) "v_add_f32 v150, v150, v151\n\tv_sqrt_f32 v150, v150\n\t" UNP(164, 142) \
    "v_add_f32 v150, 0x358637bd, v150\n\tv_rcp_f32_e64 v150, v150\n\t" UNP(168, 144)
#define PKM(x) "v_pk_mul_f32 v[" #x ":" #x "+1], v[150:151], v[" #x ":" #x "+1] op_sel_hi:[0,1]\n\t"
#define CVT(d, x) "v_cvt_pk_bf16_f32 v[" #d "], v[" #x "], v[" #x "+1]\n\t"
#define K_PK8(x) PKM(x) PKM(x + 2) PKM(x + 4) PKM(x + 6) CVT(x, x) CVT(x + 1, x + 2) CVT(x + 2, x + 4) CVT(x + 3, x + 6)
#define SKIP7(PART) "s_cbranch_scc1 .Lqk_skip_%=\n\t" NOP PART ".Lqk_skip_%=:\n\t"
#define WL(r, s) "ds_write_b16 v146, v" #r " offset:" #s "*16\n\t"
#define WH(r, s) "ds_write_b16_d16_hi v146, v" #r " offset:" #s "*16\n\t"
#define LDA "global_load_dwordx4 v[136:139], v149, s[20:21]\n\t"
#define LDB "global_load_dwordx4 v[140:143], v149, s[20:21] offset:16\n\t"
#define LDC "global_load_dword v144, v149, s[20:21] offset:32\n\t"
#define FLAG "ds_write_b32 v181, v179\n\t"
#define BODY_TOP(r) \
    ".p2align 6\n" \
    ".Lqk_top_" #r "_%=:\n\t" \
    STEP(0, 3, "", 32, 64, 104, 108, 112, 116, 124, 120, 1, "v_cmp_lt_u32_e64 vcc, v180, v179\n\t", \
         "s_cbranch_vccnz .Lqk_spin_" #r "_%=\n\t" NOP ".Lqk_go_" #r "_%=:\n\t", KXL(0), "", "", "") \
    "s_cmp_eq_u32 %[it], 8\n\ts_cbranch_scc1 .Lqk_end_" #r "_%=\n\t" \
    STEP_ODD(1, 3, "", "", "v_add_u32_e64 v179, 1, v179\n\t", "", "") \
    ".Lqk_mid_" #r "_%=:\n\t" \
    "s_cmp_eq_u32 %[it], 7\n\t" NOP
#define BODY_END(r) \
    STEP_ODD(7, 0, "", "s_cmp_eq_u32 %[it], 6\n\ts_cselect_b32 %[tmp], -7, 1\n\t", "ds_read_b32 v180, v182\n\tv_mad_i32_i24 v146, %[tmp], v177, v146\n\t", \
             "v_mad_i32_i24 v147, %[tmp], v178, v147\n\t", "v_add_u32 v133, 0x2000, v133\n\t") \
    "v_add_u32 v134, 0x400, v134\n\tv_add_u32 v135, 0x2800, v135\n\t" \
    "s_add_u32 %[it], %[it], 1\n\ts_cmp_lt_u32 %[it], 6\n\ts_cselect_b32 %[tmp], 0x2400, 0\n\t"        \
    "s_add_u32 s20, s20, %[tmp]\n\ts_addc_u32 s21, s21, 0\n\t" \
    "s_branch .Lqk_top_" #r "_%=\n" \
    ".Lqk_spin_" #r "_%=:\n\ts_setprio 0\n\tds_read_b32 v180, v182\n\ts_waitcnt lgkmcnt(0)\n\tv_cmp_lt_u32 vcc, v180, v179\n\ts_nop 4\n\t" \
    "s_cbranch_vccnz .Lqk_spin_" #r "_%=\n\ts_setprio 3\n\ts_branch .Lqk_go_" #r "_%=\n\t"
#define BODY_K BODY_TOP(k) \
    STEP_EVEN(2, 3, "vmcnt(0) ", SKIP7(K_NORM K_PK8(152) K_PK8(160) PKM(168) CVT(168, 168)), \
              "ds_write_b128 v146, v[152:155]\n\t", "ds_write_b128 v146, v[160:163] offset:512\n\t") \
    STEP_ODD(3, 3, "", "ds_write_b32 v147, v168\n\t", FLAG, LDA, LDB) \
    STEP_EVEN(4, 2, "", LDC, "", "") \
    STEP_ODD(5, 2, "", "", "", "", "") \
    STEP_EVEN(6, 1, "", "", "", "") BODY_END(k)
#define BODY_V BODY_TOP(v) \
    STEP_EVEN(2, 3, "vmcnt(2) ", WL(136, 0), WH(136, 1) WL(137, 2), WH(137, 3)) \
    STEP_ODD(3, 3, "", WL(138, 4), WH(138, 5), WL(139, 6) WH(139, 7), LDA) \
    STEP_EVEN(4, 2, "vmcnt(2) ", WL(140, 8), WH(140, 9) WL(141, 10), WH(141, 19)) \
    STEP_ODD(5, 2, "", WL(142, 12), WH(142, 13), WL(143, 14) WH(143, 15), LDB) \
    STEP_EVEN(6, 1, "vmcnt(2) ", WL(144, 16), WH(144, 17) "ds_write_b32 v147, v148\n\t", FLAG LDC) BODY_END(v)
#define OMUL(O, i) "v_pk_mul_f32 v[" #O "+" #i ":" #O "+" #i "+1], v[" #O "+10:" #O "+11], v[" #O "+" #i ":" #O "+" #i "+1] op_sel_hi:[0,1]\n\t"
#define OCVT(O, d, i) "v_cvt_pk_bf16_f32 v[" #O "+" #d "], v[" #O "+" #i "], v[" #O "+" #i "+1]\n\t"
#define OUT_HEAD(O) \
    "v_mov_b32 v[" #O "+11], v[" #O "+10]\n\ts_nop 1\n\tv_permlane32_swap_b32 v[" #O "+10], v[" #O "+11]\n\tv_rcp_f32 v[" #O "+10], v[" #O "+10]\n\ts_nop 0\n\t"
#define OUT_REST(O, A) \
    OMUL(O, 0) OMUL(O, 2) OMUL(O, 4) OMUL(O, 6) OMUL(O, 8) OCVT(O, 0, 0) OCVT(O, 1, 2) OCVT(O, 2, 4) OCVT(O, 3, 6) OCVT(O, 4, 8) \
    "ds_write2_b32 v" #A ", v[" #O "], v[" #O "+1] offset1:1\n\tds_write2_b32 v" #A ", v[" #O "+2], v[" #O "+3] offset0:4 offset1:5\n\t" \
    "s_mov_b32 exec_hi, 0\n\tds_write_b32 v" #A ", v[" #O "+4] offset:32\n\ts_mov_b32 exec_hi, -1\n\t"
#define LDROW(a, b, c, off, base) \
    "global_load_dwordx4 " a ", " off ", " base "\n\tglobal_load_dwordx4 " b ", " off ", " base " offset:16\n\tglobal_load_dword " c ", " off ", " base " offset:32\n\t"

__global__ __launch_bounds__(512) void qk_norm_attn(const unsigned* __restrict__ q, const unsigned* __restrict__ k,
                                                    const unsigned* __restrict__ v, const unsigned* __restrict__ t,
                                                    unsigned* __restrict__ out) {
    __shared__ __attribute__((aligned(16))) char smem[LDS_BYTES];
    lds_ptr lds = (lds_ptr)smem;
    const int tid = threadIdx.x, lane = tid & 63, r32 = lane & 31, hi = lane >> 5;
    const int wid = __builtin_amdgcn_readfirstlane(tid >> 6);
    const int b = blockIdx.x;
    const int head = (b & 7) + 8 * (b >> 5), qc = (b >> 3) & 3;
    const size_t hbase = (size_t)head * SEQ * 9;
    const int qrow = qc * 512 + wid * 64 + r32;
    const bool is_k = wid < 4;
    const int srow = tid & 255;
    g_ptr src = uniform_ptr((is_k ? k : v) + hbase);
    g_ptr qsrc = uniform_ptr(q + hbase);
    g_ptr dst = uniform_ptr(out + hbase + (size_t)(qc * 512 + wid * 64) * 9);
    const unsigned tw = t[(head & (NHEADS - 1)) >> 1];
    unsigned soff = srow * 36;
    const unsigned qoffh = qrow * 36 + 16 * hi, qoffx = qrow * 36 + 1152 * hi;
    Row9 raw, nxt; u32x4 qa0, qa1; unsigned qb;
    asm volatile("global_load_dword %4, %[qx], %[qs] offset:32\n\tglobal_load_dwordx4 %3, %[qh], %[qs]\n\t"
                 "global_load_dwordx4 %5, %[qh], %[qs] offset:1152\n\t" LDROW("%0", "%1", "%2", "%[so]", "%[src]")
                 : "=&v"(raw.a), "=&v"(raw.b), "=&v"(raw.c), "=&v"(qa0), "=&v"(qb), "=&v"(qa1)
                 : [so] "v"(soff), [src] "s"(src), [qh] "v"(qoffh), [qx] "v"(qoffx), [qs] "s"(qsrc) : "memory");
    int sm_step = is_k ? 8192 : 10240, sx_step = is_k ? 1024 : 10240;
    const int vrow = V_OFF + (srow >> 3) * 320;
    lds_ptr sm = lds + (is_k ? KM_OFF + (srow >> 5) * 1024 + (srow & 31) * 16 : vrow + (srow & 7) * 2);
    lds_ptr sx = lds + (is_k ? KX_OFF + srow * 4 : vrow + ((srow & 4) ? 11 * 16 : 18 * 16) + (srow & 3) * 4);
    unsigned cval = (srow & 4) ? 0u : 0x3f803f80u;
    const float c = __uint_as_float((head & 1) ? (tw & 0xffff0000u) : (tw << 16)) * LOG2E;
    const bool fast = __builtin_fabsf(c) <= SHIFT_SAFE;
    const unsigned shift_bits = fast ? (cvt_pk(-__builtin_fabsf(c), 0.f) & 0xffffu) : 0u;
    const int vslot = r32 == 11 ? 19 : (r32 > 18 ? 11 : r32);
    lds_ptr kmp = lds + KM_OFF - 2048 + lane * 16;
    lds_ptr kxp = lds + KX_OFF - 256 + lane * 4;
    lds_ptr vp = lds + V_OFF - 2560 + hi * 320 + vslot * 16;
    int sel = 0xDDDD;
    lds_ptr patch = lds + V_OFF + 20480 + wid * 2304;
    lds_ptr orow = patch + r32 * 36 + 8 * hi, olin = patch + lane * 16, fown = lds + wid * 4, fall = lds + (lane & 7) * 4;
    u32x2 ident = (u32x2){(lane & 2) ? 0u : (0x3f80u << (16 * (lane & 1))), (lane & 2) ? (0x3f80u << (16 * (lane & 1))) : 0u};
    u32x2 kx = (u32x2){0u, ONE_ZERO};
    int token = 1, zero = 0, ooff = lane * 16;
    f32x16 o0, o1; u32x8 pz; u32x4 vz;
#pragma unroll
    for (int i = 0; i < 16; ++i) { o0[i] = 0.f; o1[i] = 0.f; }
#pragma unroll
    for (int i = 0; i < 8; ++i) { pz[i] = 0u; vz[i & 3] = 0u; }
    asm volatile("" : "+{v[0:15]}"(o0), "+{v[16:31]}"(o1), "+{v[88:95]}"(pz), "+{v[116:119]}"(vz), "+{v[128:129]}"(kx), "+{v132}"(sel),
                 "+{v133}"(kmp), "+{v134}"(kxp), "+{v135}"(vp), "+{v145}"(zero), "+{v148}"(cval), "+{v149}"(soff), "+{v[172:173]}"(ident),
                 "+{v174}"(orow), "+{v175}"(olin), "+{v176}"(ooff), "+{v177}"(sm_step), "+{v178}"(sx_step), "+{v179}"(token),
                 "+{v181}"(fown), "+{v182}"(fall), "+s"(dst));
    u32x4 qm0, qm1;
    asm volatile("s_waitcnt vmcnt(4)" : "+v"(qa0), "+v"(qb));
    const float g0 = prep_q(qa0, hi ? 0u : qb, c, qm0);
    asm volatile("s_waitcnt vmcnt(3)" : "+v"(qa1));
    const float g1 = prep_q(qa1, hi ? qb : 0u, c, qm1);
    u32x2 qx = (u32x2){scale2(qb, hi ? g1 : g0), shift_bits};
    PIN(qm0); PIN(qm1); PIN(qx);
    asm volatile("s_waitcnt vmcnt(0)\n\t" LDROW("v[136:139]", "v[140:143]", "v144", "v149", "%[src]")
                 : "=&{v[136:139]}"(nxt.a), "=&{v[140:143]}"(nxt.b), "=&{v144}"(nxt.c), "+v"(raw.a), "+v"(raw.b), "+v"(raw.c)
                 : "{v149}"(soff), [src] "s"(src + 9216) : "memory");
    if (is_k) stage_k(sm, sx, raw); else stage_v(sm, sx, cval, raw);
    sm += sm_step; sx += sx_step;
    *(LDS_AS(int)*)fown = 0;
    __syncthreads();

    if (__builtin_expect(fast, 1)) {
        int tmp, it = 0;
        f32x32 sa; u32x16 sb0; u32x8 sb1, vf0, kf; u32x4 vf1;
        unsigned long long nsrc = (unsigned long long)(src + 2 * 9216);
        asm volatile(
            "ds_read_b32 v128, v134 offset:256\n\t" LD128(120, "v133", "2048") LD128(124, "v133", "3072")
            LD128(104, "v135", "2560") LD128(108, "v135", "3200")
            "s_waitcnt lgkmcnt(4)\n\t" QX(32, 0) "s_nop 15\n\ts_nop 1\n\ts_waitcnt lgkmcnt(0)\n\t" QK(32, 0, 120, 96) QK(32, 16, 120, 100) "s_nop 7\n\t"
            "s_cmp_gt_u32 %[wid], 3\n\ts_cbranch_scc1 .Lqk_mid_v_%=\n\ts_branch .Lqk_mid_k_%=\n\t"
            BODY_K BODY_V
            ".p2align 5\n"
            ".Lqk_end_k_%=:\n\ts_setprio 0\n\t" NOP
            ".Lqk_end_v_%=:\n\t"
            NOP "s_waitcnt lgkmcnt(0)\n\t" EX2(64, 0, 1) PV(16, 108, 32, 24) EX8(64, 2, 3, 4, 5, 6, 7, 8, 9) PV(0, 112, 64, 0) EX8(64, 10, 11, 12, 13, 14, 15, 16, 17)
            PV(0, 116, 64, 8) EX8(64, 18, 19, 20, 21, 22, 23, 24, 25) PV(16, 112, 64, 16) EX2(64, 26, 27) EX2(64, 28, 29) EX2(64, 30, 31)
            OUT_HEAD(0) PV(16, 116, 64, 24) OUT_REST(0, 174)
            "v_add_u32 v174, 1152, v174\n\t"
            OUT_HEAD(16) OUT_REST(16, 174)
            "s_waitcnt lgkmcnt(0)\n\t"
            "ds_read_b128 v[32:35], v175\n\tds_read_b128 v[36:39], v175 offset:1024\n\tds_read_b128 v[40:43], v175 offset:2048\n\t"
            "s_waitcnt lgkmcnt(2)\n\tglobal_store_dwordx4 v176, v[32:35], %[dst] sc1\n\t"
            "s_waitcnt lgkmcnt(1)\n\tglobal_store_dwordx4 v176, v[36:39], %[dst] offset:1024 sc1\n\t"
            "s_mov_b64 exec, 0xffff\n\t"
            "s_waitcnt lgkmcnt(0)\n\tglobal_store_dwordx4 v176, v[40:43], %[dst] offset:2048 sc1\n\t"
            "s_mov_b64 exec, -1\n\ts_nop 1"
            : "+{v[0:15]}"(o0), "+{v[16:31]}"(o1), "=&{v[32:63]}"(sa), "=&{v[64:79]}"(sb0), "=&{v[80:87]}"(sb1), "+{v[88:95]}"(pz),
              "=&{v[104:111]}"(vf0), "=&{v[112:115]}"(vf1), "+{v[116:119]}"(vz), "=&{v[120:127]}"(kf),
              "+{v[128:129]}"(kx), "+{v133}"(kmp), "+{v134}"(kxp), "+{v135}"(vp), "+{v[136:139]}"(nxt.a), "+{v[140:143]}"(nxt.b),
              "+{v144}"(nxt.c), "+{v146}"(sm), "+{v147}"(sx), "+{v174}"(orow), "+{v179}"(token), [it] "+s"(it), "+{s[20:21]}"(nsrc),
              [tmp] "=&s"(tmp)
            : "{v[96:99]}"(qm0), "{v[100:103]}"(qm1), "{v[130:131]}"(qx), "{v132}"(sel), "{v145}"(zero), "{v148}"(cval), "{v149}"(soff),
              "{v[172:173]}"(ident), "{v175}"(olin), "{v176}"(ooff), "{v177}"(sm_step), "{v178}"(sx_step),
              "{v181}"(fown), "{v182}"(fall), [dst] "s"(dst), [wid] "s"(wid)
            : "memory", "scc", "vcc", "v150", "v151", "v152", "v153", "v154", "v155", "v156", "v157", "v158", "v159", "v160", "v161",
              "v162", "v163", "v164", "v165", "v166", "v167", "v168", "v169", "v170", "v171", "v180");
    } else {
        asm volatile("s_waitcnt vmcnt(0)" :: "{v[136:139]}"(nxt.a), "{v[140:143]}"(nxt.b), "{v144}"(nxt.c) : "memory");
        float m0 = -INFINITY, m1 = -INFINITY;
        kmp += 2048; vp += 2560;
        kxp = lds + KX_OFF + r32 * 4;
        const bf16x8 q0 = __builtin_bit_cast(bf16x8, qm0), q1 = __builtin_bit_cast(bf16x8, qm1);
        const s16x4 qxs = __builtin_bit_cast(s16x4, qx);
#pragma unroll 1
        for (int i = 0; i < 64; ++i) {
            if ((i & 7) == 0 && i > 0) {
                raw = load_row(src + (i >> 3) * 9216, soff);
                if (is_k) stage_k(sm, sx, raw); else stage_v(sm, sx, cval, raw);
                sm += sm_step; sx += sx_step;
                __syncthreads();
            }
            const bf16x8 kf = LDB128(kmp), v0 = LDB128(vp), v1 = LDB128(vp + 640);
            const s16x4 kx = __builtin_bit_cast(s16x4, (u32x2){*(const LDS_AS(unsigned)*)kxp, ONE_ZERO});
            f32x32 zero32;
#pragma unroll
            for (int j = 0; j < 32; ++j) zero32[j] = 0.f;
            const f32x32 sx32 = __builtin_amdgcn_mfma_f32_32x32x4bf16_1k(kx, qxs, zero32, 0, 0, 0);
            f32x16 s0 = MF16(kf, q0, LO16(sx32)), s1 = MF16(kf, q1, HI16(sx32));
#define ONLINE(S, M, O) do { float mx = S[0]; _Pragma("unroll") for (int j = 1; j < 16; ++j) mx = __builtin_fmaxf(mx, S[j]); \
                auto sw = __builtin_amdgcn_permlane32_swap(__float_as_uint(mx), __float_as_uint(mx), false, false); \
                const float mn = __builtin_fmaxf(M, __builtin_fmaxf(__uint_as_float(sw[0]), __uint_as_float(sw[1]))); \
                const float alpha = __builtin_amdgcn_exp2f(M - mn); M = mn; \
                _Pragma("unroll") for (int j = 0; j < 16; ++j) { O[j] *= alpha; S[j] = __builtin_amdgcn_exp2f(S[j] - mn); } \
                O = SMF(v0, LO8(S), O); O = SMF(v1, HI8(S), O); } while (0)
            ONLINE(s0, m0, o0);
            ONLINE(s1, m1, o1);
            kmp += 1024; kxp += 128; vp += 1280;
        }
        put_o(patch + r32 * 36, o0, hi);
        put_o(patch + (r32 + 32) * 36, o1, hi);
        __builtin_amdgcn_fence(__ATOMIC_RELEASE, "wavefront");
        __builtin_amdgcn_wave_barrier();
        const u32x4 x0 = *(LDS_AS(u32x4)*)(patch + lane * 16), x1 = *(LDS_AS(u32x4)*)(patch + 1024 + lane * 16);
        const u32x4 x2 = *(LDS_AS(u32x4)*)(patch + 2048 + (lane & 15) * 16);
        store16(dst, lane * 16, x0);
        store16(dst, 1024 + lane * 16, x1);
        if (lane < 16) store16(dst, 2048 + lane * 16, x2);
        __builtin_amdgcn_s_waitcnt(0x0F70);
    }
}

extern "C" void kernel_launch(void* const* d_in, const int* in_sizes, int n_in, void* d_out, int out_size, void* d_ws,
                              size_t ws_size, hipStream_t stream) {
    qk_norm_attn<<<256, 512, 0, stream>>>((const unsigned*)d_in[0], (const unsigned*)d_in[1], (const unsigned*)d_in[2],
                                          (const unsigned*)d_in[3], (unsigned*)d_out);
}
```
